```python
import torch

def _softmax(x, axis=-1):
    return torch.softmax(x, dim=axis)
def _gelu(x):
    return torch.nn.functional.gelu(x, approximate="tanh")
def _silu(x):
    return torch.nn.functional.silu(x)
def _sigmoid(x):
    return torch.sigmoid(x)
def _layernorm(x, axis=-1, eps=1e-5):
    m = x.mean(dim=axis, keepdim=True)
    v = x.var(dim=axis, keepdim=True, unbiased=False)
    return (x - m) / torch.sqrt(v + eps)
def _scatter_add(tgt, idx, src, axis=0):
    return tgt.clone().index_add_(axis, idx, src)
def _segment_sum(src, seg, n):
    out = torch.zeros((n,) + src.shape[1:], dtype=src.dtype)
    return out.index_add_(0, seg, src)
def _pad(x, pw):
    flat = tuple(v for pair in reversed(pw) for v in pair)
    return torch.nn.functional.pad(x, flat)

kernel_name = 'interleave_layers__sliding_block_cached__mixer_token_mlp__decode'

def setup_inputs():

    _w = (lambda a: (lambda t: t.to(torch.bfloat16) if t.is_floating_point() else t)(torch.as_tensor(a)))
    def _A_setup():
        B, Sq, D = 1, 1, 512
        Hq, Hkv, Sk, Dh = 32, 8, 65536, 128
        assert Hq % Hkv == 0, (Hq, Hkv)
        return {
            "x": _w((lambda *s: torch.randn(*s, dtype=torch.bfloat16) * 0.05)(B, Sq, D)),
            "k_cache": _w((lambda *s: torch.randn(*s, dtype=torch.bfloat16) * 0.8)(B, Hkv, Sk, Dh)),
            "v_cache": _w((lambda *s: torch.randn(*s, dtype=torch.bfloat16) * 1.6)(B, Hkv, Sk, Dh)),
            "wq": _w((lambda *s: torch.randn(*s, dtype=torch.bfloat16) * 0.8)(D, Hq * Dh) * (D ** -0.5)),
            "wo": _w((lambda *s: torch.randn(*s, dtype=torch.bfloat16) * 1.6)(Hq * Dh, D) * ((Hq * Dh) ** -0.5)),
        }
    _A_in = _A_setup()

    def _B_setup():
        B, S, D, F = 1, 1, 512, 1792
        return {
            "x": _w((lambda *s: torch.randn(*s, dtype=torch.bfloat16) * 0.05)(B, S, D)),
            "w1": _w((lambda *s: torch.randn(*s, dtype=torch.bfloat16) * 0.8)(S, F) * (S ** -0.5)),
            "b1": _w((lambda *s: torch.randn(*s, dtype=torch.bfloat16) * 0.2)(F) * 0.02),
            "w2": _w((lambda *s: torch.randn(*s, dtype=torch.bfloat16) * 0.2)(F, S) * (F ** -0.5)),
            "b2": _w((lambda *s: torch.randn(*s, dtype=torch.bfloat16) * 0.2)(S) * 0.02),
        }
    _B_in = _B_setup()

    return {
        "A_x": _A_in["x"],
        "A_k_cache": _A_in["k_cache"],
        "A_v_cache": _A_in["v_cache"],
        "A_wq": _A_in["wq"],
        "A_wo": _A_in["wo"],
        "B_x": _B_in["x"],
        "B_w1": _B_in["w1"],
        "B_b1": _B_in["b1"],
        "B_w2": _B_in["w2"],
        "B_b2": _B_in["b2"],
    }

def reference(A_x, A_k_cache, A_v_cache, A_wq, A_wo, B_x, B_w1, B_b1, B_w2, B_b2):
    def _A_ref(x, k_cache, v_cache, wq, wo):
        B, Sq, D = x.shape
        Hq, Dh = 32, 128
        Hkv, Sk = k_cache.shape[1], k_cache.shape[2]
        W = min(4096, max(256, Sk // 4))
        rep = Hq // Hkv
        q = (lambda x, *d: x.permute(*d))((x @ wq).reshape(B, Sq, Hq, Dh), 0, 2, 1, 3)
        k = (lambda x, n, axis: torch.repeat_interleave(x, n, dim=axis))(k_cache[:, :, Sk - W:, :], rep, axis=1)
        v = (lambda x, n, axis: torch.repeat_interleave(x, n, dim=axis))(v_cache[:, :, Sk - W:, :], rep, axis=1)
        scores = torch.einsum("bhqd,bhkd->bhqk", q, k) * (Dh ** -0.5)
        p = _softmax(scores, axis=-1)
        o = torch.einsum("bhqk,bhkd->bhqd", p, v)
        return (lambda x, *d: x.permute(*d))(o, 0, 2, 1, 3).reshape(B, Sq, Hq * Dh) @ wo

    def _B_ref(x, w1, b1, w2, b2):
        B, S, D = x.shape
        h = _layernorm(x, axis=-1)
        u = torch.einsum("bsd,sf->bdf", h, w1) + b1
        u = _gelu(u)
        y = torch.einsum("bdf,fs->bds", u, w2) + b2
        return (lambda x, *d: x.permute(*d))(y, 0, 2, 1)

    x = A_x
    x = x + _A_ref(_layernorm(x, axis=-1), A_k_cache, A_v_cache, A_wq, A_wo)
    x = x + _B_ref(_layernorm(x, axis=-1), B_w1, B_b1, B_w2, B_b2)
    x = x + _A_ref(_layernorm(x, axis=-1), A_k_cache, A_v_cache, A_wq, A_wo)
    x = x + _B_ref(_layernorm(x, axis=-1), B_w1, B_b1, B_w2, B_b2)
    return x
```

```cpp
#include <hip/hip_runtime.h>
#include <hip/hip_bf16.h>
#include <stdint.h>

typedef unsigned short bf16_t;
__device__ __forceinline__ float bf2f(bf16_t b) { return __uint_as_float(((unsigned)b) << 16); }
__device__ __forceinline__ float rbf(float f) { return f;
    unsigned u = __float_as_uint(f);
    u += 0x7FFFu + ((u >> 16) & 1u);
    return __uint_as_float(u & 0xFFFF0000u);
}
__device__ __forceinline__ bf16_t f2bf(float f) {
    unsigned u = __float_as_uint(f);
    u += 0x7FFFu + ((u >> 16) & 1u);
    return (bf16_t)(u >> 16);
}

constexpr int D = 512, HQ = 32, HKV = 8, SK = 65536, DH = 128, W = 4096, F = 1792;

__device__ float block_sum(float v, float* red) {
    for (int o = 32; o >= 1; o >>= 1) v += __shfl_xor(v, o);
    int w = threadIdx.x >> 6;
    __syncthreads();
    if ((threadIdx.x & 63) == 0) red[w] = v;
    __syncthreads();
    float s = 0.f;
    for (int i = 0; i < (int)(blockDim.x >> 6); ++i) s += red[i];
    return s;
}

__device__ void layernorm512(const float* xs, float* hs, float* red) {
    int t = threadIdx.x;
    float a = xs[t], b = xs[t + 256];
    float mean = block_sum(a + b, red) * (1.0f / D);
    float da = a - mean, db = b - mean;
    float var = block_sum(da * da + db * db, red) * (1.0f / D);
    float m = rbf(mean), v = rbf(var);
    float sd = rbf(sqrtf(rbf(v + 1e-5f)));
    __syncthreads();
    hs[t] = rbf(rbf(a - m) / sd);
    hs[t + 256] = rbf(rbf(b - m) / sd);
    __syncthreads();
}

__global__ void k_init(const bf16_t* x, float* xw) { int t = threadIdx.x; xw[t] = bf2f(x[t]); }

__global__ void k_q(const float* xw, const bf16_t* wq, float* q) {
    __shared__ float xs[D], hs[D], red[8], part[16][17];
    int t = threadIdx.x;
    xs[t] = xw[t]; xs[t + 256] = xw[t + 256];
    __syncthreads();
    layernorm512(xs, hs, red);
    int col = blockIdx.x * 16 + (t & 15), rg = t >> 4;
    float acc = 0.f;
    for (int i = 0; i < 32; ++i) { int r = rg * 32 + i; acc += hs[r] * bf2f(wq[(size_t)r * (HQ * DH) + col]); }
    part[rg][t & 15] = acc;
    __syncthreads();
    if (t < 16) { float s = 0.f; for (int i = 0; i < 16; ++i) s += part[i][t]; q[blockIdx.x * 16 + t] = rbf(s); }
}

__global__ void k_attn(const float* q, const bf16_t* kc, const bf16_t* vc, float* po, float* pml) {
    __shared__ float qs[4 * DH], sc[4][128], red[8];
    int t = threadIdx.x, g = blockIdx.x & 7, s = blockIdx.x >> 3;
    qs[t] = q[g * 512 + t]; qs[t + 256] = q[g * 512 + t + 256];
    __syncthreads();
    const bf16_t* kb = kc + ((size_t)g * SK + (SK - W) + s * 128) * DH;
    const bf16_t* vb = vc + ((size_t)g * SK + (SK - W) + s * 128) * DH;
    {
        int key = t & 127, hp = t >> 7;
        float a0 = 0.f, a1 = 0.f;
        for (int d = 0; d < DH; ++d) { float kv = bf2f(kb[key * DH + d]); a0 += qs[(2 * hp) * DH + d] * kv; a1 += qs[(2 * hp + 1) * DH + d] * kv; }
        sc[2 * hp][key] = rbf(rbf(a0) * 0.08838834764831845f);
        sc[2 * hp + 1][key] = rbf(rbf(a1) * 0.08838834764831845f);
    }
    __syncthreads();
    int h = t >> 6, lane = t & 63;
    float s0 = sc[h][lane], s1 = sc[h][lane + 64];
    float m = fmaxf(s0, s1);
    for (int o = 32; o >= 1; o >>= 1) m = fmaxf(m, __shfl_xor(m, o));
    float p0 = __expf(s0 - m), p1 = __expf(s1 - m);
    float l = p0 + p1;
    for (int o = 32; o >= 1; o >>= 1) l += __shfl_xor(l, o);
    __syncthreads();
    sc[h][lane] = p0; sc[h][lane + 64] = p1;
    __syncthreads();
    float o0 = 0.f, o1 = 0.f;
    for (int k = 0; k < 128; ++k) { float p = sc[h][k]; o0 += p * bf2f(vb[k * DH + lane]); o1 += p * bf2f(vb[k * DH + lane + 64]); }
    int hh = g * 4 + h;
    po[((size_t)hh * 32 + s) * DH + lane] = o0;
    po[((size_t)hh * 32 + s) * DH + lane + 64] = o1;
    if (lane == 0) { pml[(hh * 32 + s) * 2] = m; pml[(hh * 32 + s) * 2 + 1] = l; }
}

__global__ void k_wo(const float* po, const float* pml, const bf16_t* wo, float* part) {
    __shared__ float os[16];
    int t = threadIdx.x, j = blockIdx.x, hh = j >> 3, d0 = (j & 7) * 16;
    if (t < 16) {
        float M = -INFINITY;
        for (int s = 0; s < 32; ++s) M = fmaxf(M, pml[(hh * 32 + s) * 2]);
        float L = 0.f, o = 0.f;
        for (int s = 0; s < 32; ++s) { float w = __expf(pml[(hh * 32 + s) * 2] - M); L += w * pml[(hh * 32 + s) * 2 + 1]; o += w * po[((size_t)hh * 32 + s) * DH + d0 + t]; }
        os[t] = rbf(o / L);
    }
    __syncthreads();
    for (int c = t; c < D; c += 256) {
        float a = 0.f;
        for (int r = 0; r < 16; ++r) a += os[r] * bf2f(wo[(size_t)(j * 16 + r) * D + c]);
        part[(size_t)j * D + c] = a;
    }
}

__global__ void k_red(const float* part, float* xw) {
    int c = blockIdx.x * 256 + threadIdx.x;
    float a = 0.f;
    for (int j = 0; j < 256; ++j) a += part[(size_t)j * D + c];
    xw[c] = rbf(xw[c] + rbf(a));
}

__global__ void k_mlp(const float* xin, float* xout, const bf16_t* w1, const bf16_t* b1, const bf16_t* w2, const bf16_t* b2, bf16_t* out) {
    __shared__ float xs[D], h1[D], h2[D], red[8];
    int t = threadIdx.x;
    xs[t] = xin[t]; xs[t + 256] = xin[t + 256];
    __syncthreads();
    layernorm512(xs, h1, red);
    layernorm512(h1, h2, red);
    for (int dd = 0; dd < 2; ++dd) {
        int d = blockIdx.x * 2 + dd;
        float h = h2[d], acc = 0.f;
        for (int f = t; f < F; f += 256) {
            float u = rbf(rbf(h * bf2f(w1[f])) + bf2f(b1[f]));
            float inner = 0.7978845608028654f * (u + 0.044715f * u * u * u);
            float e = __expf(2.f * inner);
            float th = 1.f - 2.f / (e + 1.f);
            float g = rbf(0.5f * u * (1.f + th));
            acc += g * bf2f(w2[f]);
        }
        float y = block_sum(acc, red);
        if (t == 0) {
            float r = rbf(xs[d] + rbf(rbf(y) + bf2f(b2[0])));
            xout[d] = r;
            if (out) out[d] = f2bf(r);
        }
    }
}

extern "C" void kernel_launch(void* const* d_in, const int* in_sizes, int n_in, void* d_out, int out_size, void* d_ws, size_t ws_size, hipStream_t stream) {
    const bf16_t* x = (const bf16_t*)d_in[0];
    const bf16_t* kc = (const bf16_t*)d_in[1];
    const bf16_t* vc = (const bf16_t*)d_in[2];
    const bf16_t* wq = (const bf16_t*)d_in[3];
    const bf16_t* wo = (const bf16_t*)d_in[4];
    const bf16_t* w1 = (const bf16_t*)d_in[6];
    const bf16_t* b1 = (const bf16_t*)d_in[7];
    const bf16_t* w2 = (const bf16_t*)d_in[8];
    const bf16_t* b2 = (const bf16_t*)d_in[9];
    float* ws = (float*)d_ws;
    float* xa = ws; float* xb = ws + 1024; float* q = ws + 2048; float* pml = ws + 8192; float* po = ws + 16384; float* part = ws + 16384 + 32 * 32 * 128;
    k_init<<<1, 512, 0, stream>>>(x, xa);
    for (int L = 0; L < 2; ++L) {
        float* xi = L == 0 ? xa : xb; float* xo = L == 0 ? xb : xa;
        k_q<<<256, 256, 0, stream>>>(xi, wq, q);
        k_attn<<<256, 256, 0, stream>>>(q, kc, vc, po, pml);
        k_wo<<<256, 256, 0, stream>>>(po, pml, wo, part);
        k_red<<<2, 256, 0, stream>>>(part, xi);
        k_mlp<<<256, 256, 0, stream>>>(xi, xo, w1, b1, w2, b2, L == 1 ? (bf16_t*)d_out : nullptr);
    }
}
```

```cpp
#include <hip/hip_runtime.h>
#include <hip/hip_bf16.h>
#include <stdint.h>

typedef unsigned short bf16_t;
typedef unsigned v4u __attribute__((ext_vector_type(4)));
typedef unsigned v2u __attribute__((ext_vector_type(2)));
typedef float f32x4 __attribute__((ext_vector_type(4)));
typedef float f32x2 __attribute__((ext_vector_type(2)));
typedef short bf16x8 __attribute__((ext_vector_type(8)));
typedef __bf16 bf16x2 __attribute__((ext_vector_type(2)));
#define LAS __attribute__((address_space(3)))

#define D_E1 6
#define D_E2 6
#define D_E3 12

constexpr int DM = 512, SK = 65536, WIN = 4096, FF = 1792;
constexpr int NWG = 256, NTHR = 576;
constexpr unsigned SENT = 0xFFFFFFFFu;
constexpr int AUX_SC1 = 16, AUX_NT = 2;
constexpr unsigned SPIN_CAP = 1u << 20;

constexpr unsigned WS_EPOCH = 0;
constexpr unsigned WS_FLAGS = 4096;
constexpr unsigned WS_TAB = 8192;
constexpr unsigned WS_QP = 16384;
constexpr unsigned QP_L = 8 * 2 * 4 * 256 * 4;
constexpr unsigned WS_OP = WS_QP + 2 * QP_L;
constexpr unsigned OP_L = 8 * 2 * 16 * 256 * 4;
constexpr unsigned WS_ML = WS_OP + 2 * OP_L;
constexpr unsigned ML_L = 8 * 2 * 16 * 4 * 4;
constexpr unsigned WS_XP = WS_ML + 2 * ML_L;
constexpr unsigned XP_L = 16 * 512 * 4;
constexpr unsigned WS_END = WS_XP + 2 * XP_L;
constexpr unsigned WS_STAMP = 1u << 20;

constexpr int L_TAB = 0;
constexpr int L_OL = 8192;
constexpr int L_WQ = 8192;
constexpr int L_ML = 8192 + 18432;
constexpr int L_QS = L_ML + 128;
constexpr int L_SL = L_QS + 512;
constexpr int L_AL = L_SL + 1024;
constexpr int L_TL = L_AL + 2048;
constexpr int L_FB = L_TL + 128;
constexpr int L_END = L_FB + 64;
constexpr int LDS_BYTES = 96 * 1024;
static_assert(L_END <= LDS_BYTES, "lds");

constexpr int TAB_E0 = 114;

__device__ __forceinline__ float bflo(unsigned p) { return __uint_as_float(p << 16); }
__device__ __forceinline__ float bfhi(unsigned p) { return __uint_as_float(p & 0xFFFF0000u); }
__device__ __forceinline__ unsigned cvtpk(float a, float b) {
    f32x2 v = {a, b};
    return __builtin_bit_cast(unsigned, __builtin_convertvector(v, bf16x2));
}
__device__ __forceinline__ float rbf(float a) { return bflo(cvtpk(a, a)); }
__device__ __forceinline__ float dot2(unsigned a, unsigned b, float c) {
    return __builtin_amdgcn_fdot2_f32_bf16(__builtin_bit_cast(bf16x2, a), __builtin_bit_cast(bf16x2, b), c, false);
}
template <int CTRL, int RM = 0xf> __device__ __forceinline__ float dpp(float x) {
    return __builtin_bit_cast(float, __builtin_amdgcn_update_dpp(0, __builtin_bit_cast(int, x), CTRL, RM, 0xf, true));
}
constexpr int ROR1 = 0x121, ROR2 = 0x122, ROR4 = 0x124, ROR8 = 0x128, QX1 = 0xB1, QX2 = 0x4E;
__device__ __forceinline__ float swap32_sum(float a, float b) {
    auto r = __builtin_amdgcn_permlane32_swap(__float_as_uint(a), __float_as_uint(b), false, false);
    return __uint_as_float(r[0]) + __uint_as_float(r[1]);
}
__device__ __forceinline__ float swap16_sum(float a, float b) {
    auto r = __builtin_amdgcn_permlane16_swap(__float_as_uint(a), __float_as_uint(b), false, false);
    return __uint_as_float(r[0]) + __uint_as_float(r[1]);
}
__device__ __forceinline__ float xq_max(float x) {
    auto s = __builtin_amdgcn_permlane16_swap(__float_as_uint(x), __float_as_uint(x), false, false);
    x = fmaxf(__uint_as_float(s[0]), __uint_as_float(s[1]));
    auto t = __builtin_amdgcn_permlane32_swap(__float_as_uint(x), __float_as_uint(x), false, false);
    return fmaxf(__uint_as_float(t[0]), __uint_as_float(t[1]));
}
__device__ __forceinline__ float xq_sum(float x) {
    x = swap16_sum(x, x);
    return swap32_sum(x, x);
}
__device__ __forceinline__ float row16_sum(float v) {
    v += dpp<ROR8>(v); v += dpp<ROR4>(v); v += dpp<ROR2>(v); v += dpp<ROR1>(v);
    return v;
}
__device__ __forceinline__ float wave_sum(float v) { return xq_sum(row16_sum(v)); }
__device__ __forceinline__ unsigned canon(float v) {
    unsigned u = __float_as_uint(v);
    return u == SENT ? 0x7FC00000u : u;
}
__device__ __forceinline__ f32x4 mfma(v4u a, v4u b, f32x4 c) {
    return __builtin_amdgcn_mfma_f32_16x16x32_bf16(__builtin_bit_cast(bf16x8, a), __builtin_bit_cast(bf16x8, b), c, 0, 0, 0);
}
__device__ __forceinline__ float gelu_tanh(float u) {
    const float c1 = -2.3022081986f, c2 = -0.1029432396f;
    float z = u * __builtin_fmaf(u * u, c2, c1);
    float e = __builtin_amdgcn_exp2f(z);
    return u * __builtin_amdgcn_rcpf(1.0f + e);
}
__device__ __forceinline__ float mlp_pair(float t, float w1l, float w1h, float b1l, float b1h, unsigned w2p, float acc) {
    unsigned p = cvtpk(t * w1l, t * w1h);
    unsigned q = cvtpk(bflo(p) + b1l, bfhi(p) + b1h);
    unsigned gp = cvtpk(gelu_tanh(bflo(q)), gelu_tanh(bfhi(q)));
    return dot2(gp, w2p, acc);
}

__device__ __forceinline__ void layernorm8(const float (&v)[8], float (&o)[8]) {
    const float piv = __builtin_bit_cast(float, __builtin_amdgcn_readfirstlane(__builtin_bit_cast(int, v[0])));
    float s1 = 0.f, s2 = 0.f;
#pragma unroll
    for (int k = 0; k < 8; ++k) { float d = v[k] - piv; s1 += d; s2 = __builtin_fmaf(d, d, s2); }
    s1 = wave_sum(s1); s2 = wave_sum(s2);
    const float md = s1 * (1.0f / DM);
    const float mean = piv + md;
    const float var = fmaxf(__builtin_fmaf(-md, md, s2 * (1.0f / DM)), 0.f);
    const unsigned mv = cvtpk(mean, var);
    const float m = bflo(mv);
    const float sd = rbf(__builtin_amdgcn_sqrtf(rbf(bfhi(mv) + 1e-5f)));
    const float r = __builtin_amdgcn_rcpf(sd);
#pragma unroll
    for (int k = 0; k < 8; k += 2) {
        unsigned p = cvtpk(v[k] - m, v[k + 1] - m);
        unsigned q = cvtpk(bflo(p) * r, bfhi(p) * r);
        o[k] = bflo(q); o[k + 1] = bfhi(q);
    }
}

#define STAMP(n) do { } while (0)

__global__ void __launch_bounds__(NTHR, 3)
fused_kernel(const bf16_t* __restrict__ x, const bf16_t* __restrict__ kc, const bf16_t* __restrict__ vc,
             const bf16_t* __restrict__ wq, const bf16_t* __restrict__ wo, const bf16_t* __restrict__ w1,
             const bf16_t* __restrict__ b1, const bf16_t* __restrict__ w2, const bf16_t* __restrict__ b2,
             bf16_t* __restrict__ out, void* ws) {
    extern __shared__ __attribute__((aligned(16))) unsigned char smem[];
    LAS unsigned char* L = (LAS unsigned char*)smem;
    const int b = blockIdx.x, tid = threadIdx.x, lane = tid & 63;
    const int wave = __builtin_amdgcn_readfirstlane(tid >> 6);
    const int g = b & 7, i = b >> 3, hp = i & 1, s = i >> 1, cb = i & 7, rb = i >> 3;
    const __amdgpu_buffer_rsrc_t rs = __builtin_amdgcn_make_buffer_rsrc(ws, 0, (int)WS_END, 0x00020000);
    const unsigned team = (unsigned)(g * 2 + hp);
    STAMP(0);

    if (wave == 8) {
        const v2u ep = __builtin_amdgcn_raw_buffer_load_b64(rs, WS_EPOCH, 0, AUX_SC1);
        const v4u sv = {SENT, SENT, SENT, SENT};
#pragma unroll
        for (int l2 = 0; l2 < 2; ++l2) {
            __builtin_amdgcn_raw_buffer_store_b128(sv, rs, WS_OP + l2 * OP_L + (team * 16 + s) * 1024 + lane * 16, 0, AUX_SC1);
            unsigned off;
            if (lane < 16) off = WS_QP + l2 * QP_L + (((g * 2 + (cb >> 2)) * 4 + rb) * 256 + (cb & 3) * 64) * 4 + lane * 16;
            else if (lane < 24) off = WS_XP + l2 * XP_L + (team * 512 + s * 32) * 4 + (lane - 16) * 16;
            else if (lane == 24) off = WS_ML + l2 * ML_L + (team * 16 + s) * 16;
            else off = WS_TAB + b * 32 + ((lane - 25) & 1) * 16;
            if (lane < 27) __builtin_amdgcn_raw_buffer_store_b128(sv, rs, off, 0, AUX_SC1);
        }
        const unsigned long long T = (((unsigned long long)ep.y << 32) | ep.x) + 0x9E3779B97F4A7C15ull;
        const unsigned tlo = __builtin_amdgcn_readfirstlane((unsigned)T), thi = __builtin_amdgcn_readfirstlane((unsigned)(T >> 32));
        asm volatile("s_waitcnt vmcnt(0)" ::: "memory");
        if (lane == 0) { v2u tv = {tlo, thi}; __builtin_amdgcn_raw_buffer_store_b64(tv, rs, WS_FLAGS + b * 8, 0, AUX_SC1); }
        __syncthreads();
        for (unsigned sp = 0; sp < SPIN_CAP; ++sp) {
            __builtin_amdgcn_s_sleep(16);
            asm volatile("" ::: "memory");
            const v4u f0 = __builtin_amdgcn_raw_buffer_load_b128(rs, WS_FLAGS + lane * 32, 0, AUX_SC1);
            const v4u f1 = __builtin_amdgcn_raw_buffer_load_b128(rs, WS_FLAGS + lane * 32 + 16, 0, AUX_SC1);
            const bool ok = f0.x == tlo && f0.y == thi && f0.z == tlo && f0.w == thi && f1.x == tlo && f1.y == thi && f1.z == tlo && f1.w == thi;
            if (__all(ok)) break;
        }
        if (b == 0 && lane == 0) { v2u tv = {tlo, thi}; __builtin_amdgcn_raw_buffer_store_b64(tv, rs, WS_EPOCH, 0, AUX_SC1); }
        __syncthreads();
        return;
    }

    const int qd = lane >> 4, c16 = lane & 15;
    unsigned xin[4];
#pragma unroll
    for (int k = 0; k < 4; ++k) xin[k] = *(const unsigned*)(x + 128 * k + 2 * lane);
    v4u wqs[2];
#pragma unroll
    for (int k = 0; k < 2; ++k)
        wqs[k] = __builtin_nontemporal_load((const v4u*)(wq + (size_t)(128 * rb + 16 * wave + (lane >> 3) + 8 * k) * 4096 + 512 * g + 64 * cb + 8 * (lane & 7)));
    const int pg = tid & 127, eg = tid >> 7;
    unsigned w1p[7], b1p[7], w2p[7];
#pragma unroll
    for (int k = 0; k < 7; ++k) {
        w1p[k] = *(const unsigned*)(w1 + 2 * (pg + 128 * k));
        b1p[k] = *(const unsigned*)(b1 + 2 * (pg + 128 * k));
        w2p[k] = *(const unsigned*)(w2 + 2 * (pg + 128 * k));
    }
    const float b2f = __uint_as_float((unsigned)b2[0] << 16);
    const size_t kvbase = ((size_t)g * SK + (SK - WIN) + 256 * s + 32 * wave) * 128;
    v4u kf[2][4];
#pragma unroll
    for (int T = 0; T < 2; ++T)
#pragma unroll
        for (int ks = 0; ks < 4; ++ks)
            kf[T][ks] = __builtin_nontemporal_load((const v4u*)(kc + kvbase + (size_t)(16 * T + c16) * 128 + 32 * ks + 8 * qd));
    __builtin_amdgcn_sched_barrier(0);

    float xr[8];
#pragma unroll
    for (int k = 0; k < 4; ++k) { xr[2 * k] = bflo(xin[k]); xr[2 * k + 1] = bfhi(xin[k]); }
    float hn[8];
    layernorm8(xr, hn);

#pragma unroll
    for (int k = 0; k < 2; ++k) *(LAS v4u*)(L + L_WQ + (16 * wave + (lane >> 3) + 8 * k) * 144 + 16 * (lane & 7)) = wqs[k];
    v4u vr[8];
#pragma unroll
    for (int j = 0; j < 8; ++j)
        vr[j] = __builtin_nontemporal_load((const v4u*)(vc + kvbase + (size_t)(16 * (j >> 2) + 4 * qd + (j & 3)) * 128 + 8 * c16));
    __syncthreads();
    unsigned wqr[8];
    {
        const v4u r0 = *(const LAS v4u*)(L + L_WQ + (2 * lane) * 144 + 16 * wave);
        const v4u r1 = *(const LAS v4u*)(L + L_WQ + (2 * lane + 1) * 144 + 16 * wave);
#pragma unroll
        for (int c = 0; c < 4; ++c) {
            wqr[2 * c] = __builtin_amdgcn_perm(r1[c], r0[c], 0x05040100);
            wqr[2 * c + 1] = __builtin_amdgcn_perm(r1[c], r0[c], 0x07060302);
        }
    }
    STAMP(1);

    auto stage_q = [&](const float (&h)[8], int layer) {
        const float h0 = rb == 0 ? h[0] : rb == 1 ? h[2] : rb == 2 ? h[4] : h[6];
        const float h1 = rb == 0 ? h[1] : rb == 1 ? h[3] : rb == 2 ? h[5] : h[7];
        const unsigned hpair = cvtpk(h0, h1);
        float a[8];
#pragma unroll
        for (int c = 0; c < 8; ++c) a[c] = dot2(wqr[c], hpair, 0.f);
#pragma unroll
        for (int c = 0; c < 4; ++c) a[c] = swap32_sum(a[c], a[c + 4]);
#pragma unroll
        for (int c = 0; c < 2; ++c) a[c] = swap16_sum(a[c], a[c + 2]);
        a[0] = row16_sum(a[0]); a[1] = row16_sum(a[1]);
        if (c16 == 0) {
            v2u o = {canon(a[0]), canon(a[1])};
            __builtin_amdgcn_raw_buffer_store_b64(o, rs, WS_QP + layer * QP_L + (((g * 2 + (cb >> 2)) * 4 + rb) * 256 + (cb & 3) * 64 + 8 * wave + 2 * qd) * 4, 0, AUX_SC1);
        }
    };
    stage_q(hn, 0);
    STAMP(2);

    {
        float w1l[7], w1h[7], b1l[7], b1h[7];
#pragma unroll
        for (int k = 0; k < 7; ++k) { w1l[k] = bflo(w1p[k]); w1h[k] = bfhi(w1p[k]); b1l[k] = bflo(b1p[k]); b1h[k] = bfhi(b1p[k]); }
        float acc[4];
#pragma unroll
        for (int u = 0; u < 4; ++u) {
            const unsigned idx = (unsigned)(16 * b + 4 * eg + u);
            const float tv = __uint_as_float((((idx >> 11) << 15) | ((idx & 0x7FFu) + (TAB_E0 << 7))) << 16);
            float a = 0.f;
#pragma unroll
            for (int k = 0; k < 7; ++k) a = mlp_pair(tv, w1l[k], w1h[k], b1l[k], b1h[k], w2p[k], a);
            acc[u] = a;
        }
        acc[0] = swap32_sum(acc[0], acc[2]); acc[1] = swap32_sum(acc[1], acc[3]);
        acc[0] = row16_sum(swap16_sum(acc[0], acc[1]));
        if (c16 == 0) *(LAS float*)(L + L_TL + (wave * 4 + qd) * 4) = acc[0];
    }
    v4u woa, wob;
    {
        const bf16_t* wp = wo + (size_t)(512 * g + 256 * hp + 32 * wave + (lane >> 2)) * 512 + 32 * s + 8 * (lane & 3);
        woa = __builtin_nontemporal_load((const v4u*)wp);
        wob = __builtin_nontemporal_load((const v4u*)(wp + 16 * 512));
    }
    __syncthreads();
    if (tid < 16) {
        const int e2 = tid >> 2, u = tid & 3;
        const float sum = *(const LAS float*)(L + L_TL + ((2 * e2) * 4 + u) * 4) + *(const LAS float*)(L + L_TL + ((2 * e2 + 1) * 4 + u) * 4);
        unsigned y = cvtpk(rbf(sum) + b2f, 0.f) & 0xFFFFu;
        if (y == 0xFFFFu) y = 0x7FC0u;
        __builtin_amdgcn_raw_buffer_store_b16((unsigned short)y, rs, WS_TAB + (16 * b + tid) * 2, 0, AUX_SC1);
    }
    STAMP(3);

    v4u vf[8];
    unsigned wop[8];

#pragma unroll 1
    for (int layer = 0; layer < 2; ++layer) {
        if (wave < 4) {
            const unsigned base = WS_QP + layer * QP_L + team * 4096 + tid * 4;
            unsigned qv[4];
            for (unsigned sp = 0; sp < SPIN_CAP; ++sp) {
                asm volatile("" ::: "memory");
#pragma unroll
                for (int k = 0; k < 4; ++k) qv[k] = __builtin_amdgcn_raw_buffer_load_b32(rs, base + k * 1024, 0, AUX_SC1);
                const bool ok = qv[0] != SENT && qv[1] != SENT && qv[2] != SENT && qv[3] != SENT;
                if (__all(ok)) break;
                __builtin_amdgcn_s_sleep(2);
            }
            const float q = (__uint_as_float(qv[0]) + __uint_as_float(qv[1])) + (__uint_as_float(qv[2]) + __uint_as_float(qv[3]));
            *(LAS unsigned short*)(L + L_QS + tid * 2) = (unsigned short)cvtpk(q, q);
        }
        __syncthreads();
        STAMP(4 + 8 * layer);
        {
            v4u qf[4];
#pragma unroll
            for (int ks = 0; ks < 4; ++ks) qf[ks] = *(const LAS v4u*)(L + L_QS + ((lane & 1) * 128 + 32 * ks + 8 * qd) * 2);
            f32x4 d0 = {0.f, 0.f, 0.f, 0.f}, d1 = {0.f, 0.f, 0.f, 0.f};
#pragma unroll
            for (int ks = 0; ks < 4; ++ks) { d0 = mfma(kf[0][ks], qf[ks], d0); d1 = mfma(kf[1][ks], qf[ks], d1); }
            float sc[8];
#pragma unroll
            for (int r = 0; r < 4; r += 2) {
                unsigned p0 = cvtpk(d0[r], d0[r + 1]), p1 = cvtpk(d1[r], d1[r + 1]);
                p0 = cvtpk(bflo(p0) * 0.08838834764831845f, bfhi(p0) * 0.08838834764831845f);
                p1 = cvtpk(bflo(p1) * 0.08838834764831845f, bfhi(p1) * 0.08838834764831845f);
                sc[r] = bflo(p0); sc[r + 1] = bfhi(p0); sc[4 + r] = bflo(p1); sc[5 + r] = bfhi(p1);
            }
            float m = fmaxf(fmaxf(fmaxf(sc[0], sc[1]), fmaxf(sc[2], sc[3])), fmaxf(fmaxf(sc[4], sc[5]), fmaxf(sc[6], sc[7])));
            m = xq_max(m) * 1.4426950408889634f;
            float lsum = 0.f;
#pragma unroll
            for (int j = 0; j < 8; ++j) { sc[j] = __builtin_amdgcn_exp2f(__builtin_fmaf(sc[j], 1.4426950408889634f, -m)); lsum += sc[j]; }
            lsum = xq_sum(lsum);
            v4u pf;
#pragma unroll
            for (int j = 0; j < 4; ++j) {
                const unsigned hi = cvtpk(sc[2 * j], sc[2 * j + 1]);
                const unsigned lo = cvtpk(sc[2 * j] - bflo(hi), sc[2 * j + 1] - bfhi(hi));
                pf[j] = (lane & 2) ? lo : hi;
            }
            if (layer == 0) {
#pragma unroll
                for (int t = 0; t < 8; ++t)
#pragma unroll
                    for (int jp = 0; jp < 4; ++jp)
                        vf[t][jp] = __builtin_amdgcn_perm(vr[2 * jp + 1][t >> 1], vr[2 * jp][t >> 1], (t & 1) ? 0x07060302u : 0x05040100u);
            }
            f32x4 o[8];
#pragma unroll
            for (int t = 0; t < 8; ++t) { f32x4 z = {0.f, 0.f, 0.f, 0.f}; o[t] = mfma(pf, vf[t], z); }
            if (lane < 16) {
#pragma unroll
                for (int h = 0; h < 2; ++h) {
                    f32x4 lo = {o[0][h] + o[0][h + 2], o[1][h] + o[1][h + 2], o[2][h] + o[2][h + 2], o[3][h] + o[3][h + 2]};
                    f32x4 hi = {o[4][h] + o[4][h + 2], o[5][h] + o[5][h + 2], o[6][h] + o[6][h + 2], o[7][h] + o[7][h + 2]};
                    *(LAS f32x4*)(L + L_OL + ((wave * 2 + h) * 128 + 8 * lane) * 4) = lo;
                    *(LAS f32x4*)(L + L_OL + ((wave * 2 + h) * 128 + 8 * lane + 4) * 4) = hi;
                }
                if (lane < 2) { f32x2 ml = {m, lsum}; *(LAS f32x2*)(L + L_ML + (lane * 8 + wave) * 8) = ml; }
            }
        }
        __syncthreads();
        if (wave < 4) {
            const int h = wave >> 1;
            f32x4 ml[4];
#pragma unroll
            for (int k = 0; k < 4; ++k) ml[k] = *(const LAS f32x4*)(L + L_ML + h * 64 + k * 16);
            float ov[8];
#pragma unroll
            for (int w = 0; w < 8; ++w) ov[w] = *(const LAS float*)(L + L_OL + (w * 256 + tid) * 4);
            float M = fmaxf(fmaxf(fmaxf(ml[0][0], ml[0][2]), fmaxf(ml[1][0], ml[1][2])), fmaxf(fmaxf(ml[2][0], ml[2][2]), fmaxf(ml[3][0], ml[3][2])));
            float o = 0.f, Ls = 0.f;
#pragma unroll
            for (int k = 0; k < 4; ++k) {
                const float f0 = __builtin_amdgcn_exp2f(ml[k][0] - M), f1 = __builtin_amdgcn_exp2f(ml[k][2] - M);
                o = __builtin_fmaf(f0, ov[2 * k], o); o = __builtin_fmaf(f1, ov[2 * k + 1], o);
                Ls = __builtin_fmaf(f0, ml[k][1], Ls); Ls = __builtin_fmaf(f1, ml[k][3], Ls);
            }
            __builtin_amdgcn_raw_buffer_store_b32(canon(o), rs, WS_OP + layer * OP_L + (team * 16 + s) * 1024 + tid * 4, 0, AUX_SC1);
            if ((tid & 127) == 0) { v2u mv = {canon(M), canon(Ls)}; __builtin_amdgcn_raw_buffer_store_b64(mv, rs, WS_ML + layer * ML_L + (team * 16 + s) * 16 + h * 8, 0, AUX_SC1); }
        }
        STAMP(5 + 8 * layer);
        if (layer == 0) {
            v4u tv;
            for (unsigned sp = 0; sp < SPIN_CAP; ++sp) {
                asm volatile("" ::: "memory");
                tv = __builtin_amdgcn_raw_buffer_load_b128(rs, WS_TAB + tid * 16, 0, AUX_SC1);
                bool ok = true;
#pragma unroll
                for (int k = 0; k < 4; ++k) ok = ok && (tv[k] & 0xFFFFu) != 0xFFFFu && (tv[k] >> 16) != 0xFFFFu;
                if (__all(ok)) break;
                __builtin_amdgcn_s_sleep(2);
            }
            *(LAS v4u*)(L + L_TAB + tid * 16) = tv;
#pragma unroll
            for (int c = 0; c < 4; ++c) {
                wop[2 * c] = __builtin_amdgcn_perm(wob[c], woa[c], 0x05040100);
                wop[2 * c + 1] = __builtin_amdgcn_perm(wob[c], woa[c], 0x07060302);
            }
        } else {
            __builtin_amdgcn_s_sleep(D_E2);
        }
        {
            const int r4 = lane >> 2, q4 = lane & 3, h = wave >> 2;
            const unsigned ob = WS_OP + layer * OP_L + (team * 16 + 4 * q4) * 1024 + (32 * wave + r4) * 4;
            const unsigned mb = WS_ML + layer * ML_L + (team * 16 + 4 * q4) * 16 + h * 8;
            unsigned oa[4], obv[4]; v2u ml[4];
            for (unsigned sp = 0; sp < SPIN_CAP; ++sp) {
                asm volatile("" ::: "memory");
                bool ok = true;
#pragma unroll
                for (int k = 0; k < 4; ++k) {
                    oa[k] = __builtin_amdgcn_raw_buffer_load_b32(rs, ob + k * 1024, 0, AUX_SC1);
                    obv[k] = __builtin_amdgcn_raw_buffer_load_b32(rs, ob + k * 1024 + 64, 0, AUX_SC1);
                    ml[k] = __builtin_amdgcn_raw_buffer_load_b64(rs, mb + k * 16, 0, AUX_SC1);
                }
#pragma unroll
                for (int k = 0; k < 4; ++k) ok = ok && oa[k] != SENT && obv[k] != SENT && ml[k].x != SENT && ml[k].y != SENT;
                if (__all(ok)) break;
                __builtin_amdgcn_s_sleep(2);
            }
            STAMP(6 + 8 * layer);
            float M = fmaxf(fmaxf(__uint_as_float(ml[0].x), __uint_as_float(ml[1].x)), fmaxf(__uint_as_float(ml[2].x), __uint_as_float(ml[3].x)));
            M = fmaxf(M, dpp<QX1>(M)); M = fmaxf(M, dpp<QX2>(M));
            float Ls = 0.f, va = 0.f, vb = 0.f;
#pragma unroll
            for (int k = 0; k < 4; ++k) {
                const float f = __builtin_amdgcn_exp2f(__uint_as_float(ml[k].x) - M);
                Ls = __builtin_fmaf(f, __uint_as_float(ml[k].y), Ls);
                va = __builtin_fmaf(f, __uint_as_float(oa[k]), va);
                vb = __builtin_fmaf(f, __uint_as_float(obv[k]), vb);
            }
            Ls += dpp<QX1>(Ls); va += dpp<QX1>(va); vb += dpp<QX1>(vb);
            Ls += dpp<QX2>(Ls); va += dpp<QX2>(va); vb += dpp<QX2>(vb);
            const float rl = __builtin_amdgcn_rcpf(Ls);
            const unsigned opair = cvtpk(va * rl, vb * rl);
            float a[8];
#pragma unroll
            for (int c = 0; c < 8; ++c) a[c] = dot2(wop[c], opair, 0.f);
#pragma unroll
            for (int c = 0; c < 4; ++c) a[c] = swap32_sum(a[c], a[c + 4]);
#pragma unroll
            for (int c = 0; c < 2; ++c) a[c] = swap16_sum(a[c], a[c + 2]);
#pragma unroll
            for (int c = 0; c < 2; ++c) { a[c] += dpp<ROR8>(a[c]); a[c] += dpp<ROR4>(a[c]); }
            if ((lane & 12) == 0) { f32x2 v = {a[0], a[1]}; *(LAS f32x2*)(L + L_SL + (wave * 32 + 8 * q4 + 2 * qd) * 4) = v; }
        }
        __syncthreads();
        if (tid < 32) {
            float sum = 0.f;
#pragma unroll
            for (int w = 0; w < 8; ++w) sum += *(const LAS float*)(L + L_SL + (w * 32 + tid) * 4);
            __builtin_amdgcn_raw_buffer_store_b32(canon(sum), rs, WS_XP + layer * XP_L + (team * 512 + 32 * s + tid) * 4, 0, AUX_SC1);
        }
        STAMP(7 + 8 * layer);
        if (layer == 1 && b != 0) return;
        __builtin_amdgcn_s_sleep(D_E3);
        {
            const unsigned xb = WS_XP + layer * XP_L + (4 * qd * 512 + 64 * wave + 4 * c16) * 4;
            v4u xv[4];
            for (unsigned sp = 0; sp < SPIN_CAP; ++sp) {
                asm volatile("" ::: "memory");
                bool ok = true;
#pragma unroll
                for (int k = 0; k < 4; ++k) xv[k] = __builtin_amdgcn_raw_buffer_load_b128(rs, xb + k * 2048, 0, AUX_SC1);
#pragma unroll
                for (int k = 0; k < 4; ++k) ok = ok && xv[k].x != SENT && xv[k].y != SENT && xv[k].z != SENT && xv[k].w != SENT;
                if (__all(ok)) break;
                __builtin_amdgcn_s_sleep(2);
            }
            float v[4];
#pragma unroll
            for (int c = 0; c < 4; ++c) v[c] = (__uint_as_float(xv[0][c]) + __uint_as_float(xv[1][c])) + (__uint_as_float(xv[2][c]) + __uint_as_float(xv[3][c]));
            const float a0 = swap32_sum(v[0], v[2]), a1 = swap32_sum(v[1], v[3]);
            const float a = swap16_sum(a0, a1);
            *(LAS float*)(L + L_AL + (64 * wave + 4 * c16 + qd) * 4) = rbf(a);
        }
        __syncthreads();
        STAMP(8 + 8 * layer);
        {
#pragma unroll
            for (int k = 0; k < 4; ++k) {
                const f32x2 av = *(const LAS f32x2*)(L + L_AL + (128 * k + 2 * lane) * 4);
                const unsigned p = cvtpk(xr[2 * k] + av[0], xr[2 * k + 1] + av[1]);
                xr[2 * k] = bflo(p); xr[2 * k + 1] = bfhi(p);
            }
            float a1[8], h2[8];
            layernorm8(xr, a1);
            layernorm8(a1, h2);
            float y[8];
            bool oor = false;
            unsigned oorm = 0;
#pragma unroll
            for (int k = 0; k < 8; ++k) {
                const unsigned u = __float_as_uint(h2[k]) >> 16;
                const unsigned rel = (u & 0x7FFFu) - (unsigned)(TAB_E0 << 7);
                const bool o = rel >= 2048u;
                oor = oor || o; oorm |= o ? (1u << k) : 0u;
                const unsigned idx = ((u >> 15) << 11) | (rel & 2047u);
                y[k] = __uint_as_float((unsigned)*(const LAS unsigned short*)(L + L_TAB + idx * 2) << 16);
            }
            if (__any(oor)) {
#pragma unroll 1
                for (int k = 0; k < 8; ++k) {
                    float hk = h2[0];
#pragma unroll
                    for (int kk = 1; kk < 8; ++kk) hk = (k == kk) ? h2[kk] : hk;
                    unsigned long long mk = __ballot((oorm >> k) & 1u);
                    while (mk) {
                        const int ln = __builtin_ctzll(mk);
                        mk &= mk - 1;
                        const float tv = __builtin_bit_cast(float, __builtin_amdgcn_readlane(__builtin_bit_cast(int, hk), ln));
                        float a = 0.f;
                        for (int p = tid; p < FF / 2; p += 512) {
                            const unsigned ww1 = *(const unsigned*)(w1 + 2 * p), bb1 = *(const unsigned*)(b1 + 2 * p), ww2 = *(const unsigned*)(w2 + 2 * p);
                            a = mlp_pair(tv, bflo(ww1), bfhi(ww1), bflo(bb1), bfhi(bb1), ww2, a);
                        }
                        a = wave_sum(a);
                        __syncthreads();
                        if (lane == 0) *(LAS float*)(L + L_FB + wave * 4) = a;
                        __syncthreads();
                        float sum = 0.f;
#pragma unroll
                        for (int w = 0; w < 8; ++w) sum += *(const LAS float*)(L + L_FB + w * 4);
                        const float yv = rbf(rbf(sum) + b2f);
#pragma unroll
                        for (int kk = 0; kk < 8; ++kk) y[kk] = (k == kk && lane == ln) ? yv : y[kk];
                    }
                }
            }
#pragma unroll
            for (int k = 0; k < 8; k += 2) {
                const unsigned p = cvtpk(xr[k] + y[k], xr[k + 1] + y[k + 1]);
                xr[k] = bflo(p); xr[k + 1] = bfhi(p);
                if (layer == 1 && wave == 0) *(unsigned*)(out + 64 * k + 2 * lane) = p;
            }
        }
        if (layer == 0) {
            layernorm8(xr, hn);
            stage_q(hn, 1);
            STAMP(9);
            __builtin_amdgcn_s_sleep(D_E1);
        }
    }
    STAMP(17);
}

extern "C" void kernel_launch(void* const* d_in, const int* in_sizes, int n_in, void* d_out, int out_size, void* d_ws,
                              size_t ws_size, hipStream_t stream) {
    const bf16_t* x = (const bf16_t*)d_in[0];
    const bf16_t* kc = (const bf16_t*)d_in[1];
    const bf16_t* vc = (const bf16_t*)d_in[2];
    const bf16_t* wq = (const bf16_t*)d_in[3];
    const bf16_t* wo = (const bf16_t*)d_in[4];
    const bf16_t* w1 = (const bf16_t*)d_in[6];
    const bf16_t* b1 = (const bf16_t*)d_in[7];
    const bf16_t* w2 = (const bf16_t*)d_in[8];
    const bf16_t* b2 = (const bf16_t*)d_in[9];
    (void)hipFuncSetAttribute((const void*)fused_kernel, hipFuncAttributeMaxDynamicSharedMemorySize, LDS_BYTES);
    fused_kernel<<<NWG, NTHR, LDS_BYTES, stream>>>(x, kc, vc, wq, wo, w1, b1, w2, b2, (bf16_t*)d_out, d_ws);
}
```

```cpp
#include <hip/hip_runtime.h>
#include <hip/hip_bf16.h>
#include <stdint.h>

typedef unsigned short bf16_t;
typedef unsigned v4u __attribute__((ext_vector_type(4)));
typedef unsigned v2u __attribute__((ext_vector_type(2)));
typedef float f32x4 __attribute__((ext_vector_type(4)));
typedef float f32x2 __attribute__((ext_vector_type(2)));
typedef short bf16x8 __attribute__((ext_vector_type(8)));
typedef short v4s __attribute__((ext_vector_type(4)));
typedef __bf16 bf16x2 __attribute__((ext_vector_type(2)));
#define LAS __attribute__((address_space(3)))

#define D_E1 6
#define D_E2 6
#define D_E3 12

constexpr int DM = 512, SK = 65536, WIN = 4096, FF = 1792;
constexpr int NWG = 256, NTHR = 576;
constexpr unsigned SENT = 0xFFFFFFFFu;
constexpr int AUX_SC1 = 16, AUX_NT = 2;
constexpr unsigned SPIN_CAP = 1u << 20;

constexpr unsigned WS_EPOCH = 0;
constexpr unsigned WS_FLAGS = 4096;
constexpr unsigned WS_TAB = 8192;
constexpr unsigned WS_QP = 16384;
constexpr unsigned QP_L = 8 * 2 * 4 * 256 * 4;
constexpr unsigned WS_OP = WS_QP + 2 * QP_L;
constexpr unsigned OP_L = 8 * 2 * 16 * 256 * 4;
constexpr unsigned WS_ML = WS_OP + 2 * OP_L;
constexpr unsigned ML_L = 8 * 2 * 16 * 4 * 4;
constexpr unsigned WS_XP = WS_ML + 2 * ML_L;
constexpr unsigned XP_L = 16 * 512 * 4;
constexpr unsigned WS_END = WS_XP + 2 * XP_L;
constexpr unsigned WS_STAMP = 1u << 20;

constexpr int L_TAB = 0;
constexpr int L_OL = 8192;
constexpr int L_ML = L_OL + 8192;
constexpr int L_QS = L_ML + 128;
constexpr int L_PL = L_QS + 768;
constexpr int L_OB = L_PL + 2048;
constexpr int L_AL = L_OB + 512;
constexpr int L_TL = L_AL + 2048;
constexpr int L_FB = L_TL + 128;
constexpr int L_FH = L_FB + 64;
constexpr int L_WQ = L_FH + 2048;
constexpr int L_WO = L_WQ + 128 * 144;
constexpr int L_END = L_WO + 256 * 80;
constexpr int LDS_BYTES = 96 * 1024;
static_assert(L_END <= LDS_BYTES, "lds");
static_assert(L_QS % 16 == 0 && L_PL % 16 == 0 && L_OB % 16 == 0 && L_AL % 16 == 0 && L_WQ % 16 == 0 && L_WO % 16 == 0 && L_FH % 16 == 0, "align");

constexpr int TAB_E0 = 114;

#define FENCE() asm volatile("" ::: "memory")

__device__ __forceinline__ float bflo(unsigned p) { return __uint_as_float(p << 16); }
__device__ __forceinline__ float bfhi(unsigned p) { return __uint_as_float(p & 0xFFFF0000u); }
__device__ __forceinline__ unsigned cvtpk(float a, float b) {
    f32x2 v = {a, b};
    return __builtin_bit_cast(unsigned, __builtin_convertvector(v, bf16x2));
}
__device__ __forceinline__ float rbf(float a) { return bflo(cvtpk(a, a)); }
__device__ __forceinline__ float dot2(unsigned a, unsigned b, float c) {
    return __builtin_amdgcn_fdot2_f32_bf16(__builtin_bit_cast(bf16x2, a), __builtin_bit_cast(bf16x2, b), c, false);
}
template <int CTRL> __device__ __forceinline__ float dpp(float x) {
    return __builtin_bit_cast(float, __builtin_amdgcn_mov_dpp(__builtin_bit_cast(int, x), CTRL, 0xf, 0xf, true));
}
constexpr int ROR1 = 0x121, ROR2 = 0x122, ROR4 = 0x124, ROR8 = 0x128;
__device__ __forceinline__ float swap32_sum(float a, float b) {
    auto r = __builtin_amdgcn_permlane32_swap(__float_as_uint(a), __float_as_uint(b), false, false);
    return __uint_as_float(r[0]) + __uint_as_float(r[1]);
}
__device__ __forceinline__ float swap16_sum(float a, float b) {
    auto r = __builtin_amdgcn_permlane16_swap(__float_as_uint(a), __float_as_uint(b), false, false);
    return __uint_as_float(r[0]) + __uint_as_float(r[1]);
}
__device__ __forceinline__ float swap32_max(float a) {
    auto r = __builtin_amdgcn_permlane32_swap(__float_as_uint(a), __float_as_uint(a), false, false);
    float o;
    asm("v_max_f32 %0, %1, %2" : "=v"(o) : "v"(r[0]), "v"(r[1]));
    return o;
}
__device__ __forceinline__ float row16_sum(float v) {
    v += dpp<ROR8>(v); v += dpp<ROR4>(v); v += dpp<ROR2>(v); v += dpp<ROR1>(v);
    return v;
}
#define MAX_DPP(v, CTRL) asm volatile("s_nop 1\n\tv_max_f32_dpp %0, %0, %0 " CTRL " row_mask:0xf bank_mask:0xf" : "+v"(v))
__device__ __forceinline__ float row16_max(float v) {
    MAX_DPP(v, "row_ror:8"); MAX_DPP(v, "row_ror:4"); MAX_DPP(v, "row_ror:2"); MAX_DPP(v, "row_ror:1");
    return v;
}
__device__ __forceinline__ float row8_max(float v) {
    MAX_DPP(v, "row_ror:4"); MAX_DPP(v, "row_ror:2"); MAX_DPP(v, "row_ror:1");
    return v;
}
__device__ __forceinline__ float wave_sum(float v) { v = row16_sum(v); v = swap16_sum(v, v); return swap32_sum(v, v); }
__device__ __forceinline__ unsigned canon(float v) {
    unsigned u = __float_as_uint(v);
    return u < SENT ? u : 0x7FC00000u;
}
__device__ __forceinline__ unsigned umax3(unsigned a, unsigned b, unsigned c) { return max(max(a, b), c); }
__device__ __forceinline__ unsigned umax4(v4u a) { return max(umax3(a.x, a.y, a.z), a.w); }
__device__ __forceinline__ f32x4 mfma(v4u a, v4u b, f32x4 c) {
    return __builtin_amdgcn_mfma_f32_16x16x32_bf16(__builtin_bit_cast(bf16x8, a), __builtin_bit_cast(bf16x8, b), c, 0, 0, 0);
}
__device__ __forceinline__ float mlp_pair(float t, f32x2 w1, f32x2 b1, unsigned w2p, float acc) {
    const f32x2 c1 = {-2.3022081986f, -2.3022081986f}, c2 = {-0.1029432396f, -0.1029432396f};
    const f32x2 tt = {t, t};
    const f32x2 a = tt * w1;
    const unsigned p = cvtpk(a.x, a.y);
    f32x2 u = {bflo(p), bfhi(p)};
    u += b1;
    const unsigned q = cvtpk(u.x, u.y);
    u.x = bflo(q); u.y = bfhi(q);
    const f32x2 z = u * __builtin_elementwise_fma(u * u, c2, c1);
    f32x2 e = {__builtin_amdgcn_exp2f(z.x), __builtin_amdgcn_exp2f(z.y)};
    e += 1.0f;
    const f32x2 r = {__builtin_amdgcn_rcpf(e.x), __builtin_amdgcn_rcpf(e.y)};
    const f32x2 gl = u * r;
    return dot2(cvtpk(gl.x, gl.y), w2p, acc);
}

__device__ __forceinline__ f32x2 unpk(unsigned p) { return f32x2{bflo(p), bfhi(p)}; }
__device__ __forceinline__ v4u layernorm_pk(v4u xp) {
    f32x2 d[4];
#pragma unroll
    for (int k = 0; k < 4; ++k) d[k] = unpk(xp[k]);
    const f32x2 s = (d[0] + d[1]) + (d[2] + d[3]);
    const float mean = wave_sum(s.x + s.y) * (1.0f / DM);
    const float m = rbf(mean);
    const f32x2 mm = {m, m};
#pragma unroll
    for (int k = 0; k < 4; ++k) d[k] -= mm;
    f32x2 q = d[0] * d[0];
#pragma unroll
    for (int k = 1; k < 4; ++k) q = __builtin_elementwise_fma(d[k], d[k], q);
    const float dm = mean - m;
    const float var = fmaxf(__builtin_fmaf(-dm, dm, wave_sum(q.x + q.y) * (1.0f / DM)), 0.f);
    const float sd = rbf(__builtin_amdgcn_sqrtf(rbf(rbf(var) + 1e-5f)));
    const float r = __builtin_amdgcn_rcpf(sd);
    const f32x2 rr = {r, r};
    v4u o;
#pragma unroll
    for (int k = 0; k < 4; ++k) {
        const f32x2 t = unpk(cvtpk(d[k].x, d[k].y)) * rr;
        o[k] = cvtpk(t.x, t.y);
    }
    return o;
}

#define STAMP(n) do { } while (0)
#define STAMP_DUMP() do { } while (0)
#define WAITVM(n) do { } while (0)

__global__ void __launch_bounds__(NTHR, 3)
fused_kernel(const bf16_t* x, const bf16_t* kc, const bf16_t* vc, const bf16_t* wq, const bf16_t* wo, const bf16_t* w1,
             const bf16_t* b1, const bf16_t* w2, const bf16_t* b2, bf16_t* out, void* ws) {
    extern __shared__ __attribute__((aligned(16))) unsigned char smem[];
    LAS unsigned char* L = (LAS unsigned char*)smem;
    const int b = blockIdx.x, tid = threadIdx.x, lane = tid & 63;
    const int wave = __builtin_amdgcn_readfirstlane(tid >> 6);
    const int g = b & 7, i = b >> 3, hp = i & 1, s = i >> 1, cb = i & 7, rb = i >> 3;
    const __amdgpu_buffer_rsrc_t rs = __builtin_amdgcn_make_buffer_rsrc(ws, 0, (int)WS_END, 0x00020000);
    const unsigned team = (unsigned)(g * 2 + hp);
    STAMP(0);

    auto gather_q = [&](int layer) {
        const unsigned base = WS_QP + layer * QP_L + team * 4096 + lane * 16;
        v4u qv[4];
#pragma unroll 1
        for (unsigned sp = 0; sp < SPIN_CAP; ++sp) {
            FENCE();
#pragma unroll
            for (int k = 0; k < 4; ++k) qv[k] = __builtin_amdgcn_raw_buffer_load_b128(rs, base + k * 1024, 0, AUX_SC1);
            const unsigned mx = max(max(umax4(qv[0]), umax4(qv[1])), max(umax4(qv[2]), umax4(qv[3])));
            if (__all(mx != SENT)) break;
            __builtin_amdgcn_s_sleep(2);
        }
        const f32x4 q = (__builtin_bit_cast(f32x4, qv[0]) + __builtin_bit_cast(f32x4, qv[1])) + (__builtin_bit_cast(f32x4, qv[2]) + __builtin_bit_cast(f32x4, qv[3]));
        *(LAS v2u*)(L + L_QS + lane * 8) = v2u{cvtpk(q[0], q[1]), cvtpk(q[2], q[3])};
    };

    if (wave == 8) {
        const v2u ep = __builtin_amdgcn_raw_buffer_load_b64(rs, WS_EPOCH, 0, AUX_SC1);
        const v4u sv = {SENT, SENT, SENT, SENT};
#pragma unroll
        for (int l2 = 0; l2 < 2; ++l2) {
            __builtin_amdgcn_raw_buffer_store_b128(sv, rs, WS_OP + l2 * OP_L + (team * 16 + s) * 1024 + lane * 16, 0, AUX_SC1);
            unsigned off;
            if (lane < 16) off = WS_QP + l2 * QP_L + (((g * 2 + (cb >> 2)) * 4 + rb) * 256 + (cb & 3) * 64) * 4 + lane * 16;
            else if (lane < 24) off = WS_XP + l2 * XP_L + (team * 512 + s * 32) * 4 + (lane - 16) * 16;
            else if (lane == 24) off = WS_ML + l2 * ML_L + (team * 16 + s) * 16;
            else off = WS_TAB + b * 32 + ((lane - 25) & 1) * 16;
            if (lane < 27) __builtin_amdgcn_raw_buffer_store_b128(sv, rs, off, 0, AUX_SC1);
        }
        *(LAS unsigned*)(L + L_QS + 512 + lane * 4) = 0u;
        const unsigned long long T = (((unsigned long long)ep.y << 32) | ep.x) + 0x9E3779B97F4A7C15ull;
        const unsigned tlo = __builtin_amdgcn_readfirstlane((unsigned)T), thi = __builtin_amdgcn_readfirstlane((unsigned)(T >> 32));
        asm volatile("s_waitcnt vmcnt(0)" ::: "memory");
        STAMP(20);
        if (lane == 0) __builtin_amdgcn_raw_buffer_store_b64(v2u{tlo, thi}, rs, WS_FLAGS + b * 8, 0, AUX_SC1);
        __syncthreads();
#pragma unroll 1
        for (unsigned sp = 0; sp < SPIN_CAP; ++sp) {
            __builtin_amdgcn_s_sleep(16);
            FENCE();
            const v4u f0 = __builtin_amdgcn_raw_buffer_load_b128(rs, WS_FLAGS + lane * 32, 0, AUX_SC1);
            const v4u f1 = __builtin_amdgcn_raw_buffer_load_b128(rs, WS_FLAGS + lane * 32 + 16, 0, AUX_SC1);
            const unsigned bad = (f0.x ^ tlo) | (f0.y ^ thi) | (f0.z ^ tlo) | (f0.w ^ thi) | (f1.x ^ tlo) | (f1.y ^ thi) | (f1.z ^ tlo) | (f1.w ^ thi);
            if (__all(bad == 0u)) break;
        }
        if (b == 0 && lane == 0) __builtin_amdgcn_raw_buffer_store_b64(v2u{tlo, thi}, rs, WS_EPOCH, 0, AUX_SC1);
        STAMP(21);
        gather_q(0);
        STAMP(22);
        __syncthreads();
        STAMP_DUMP();
        return;
    }

    const int qd = lane >> 4, c16 = lane & 15;
    const int lg = 16 * (c16 >> 2) + 4 * (c16 & 3) + qd;
    v4u xp = *(const v4u*)(x + 8 * lg);
    const __amdgpu_buffer_rsrc_t rwq = __builtin_amdgcn_make_buffer_rsrc((void*)(wq + (size_t)(128 * rb) * 4096 + 512 * g + 64 * cb), 0, 128 * 8192, 0x00020000);
    v4u wqs[2];
#pragma unroll
    for (int k = 0; k < 2; ++k) wqs[k] = __builtin_amdgcn_raw_buffer_load_b128(rwq, (16 * wave + (lane >> 3) + 8 * k) * 8192 + 16 * (lane & 7), 0, AUX_NT);
    const int pg = tid & 127, eg = tid >> 7;
    unsigned w1p[7], b1p[7], w2p[7];
#pragma unroll
    for (int k = 0; k < 7; ++k) {
        w1p[k] = *(const unsigned*)(w1 + 2 * (pg + 128 * k));
        b1p[k] = *(const unsigned*)(b1 + 2 * (pg + 128 * k));
        w2p[k] = *(const unsigned*)(w2 + 2 * (pg + 128 * k));
    }
    const float b2f = __uint_as_float((unsigned)b2[0] << 16);
    const size_t kvbase = ((size_t)g * SK + (SK - WIN) + 256 * s) * 128;
    const __amdgpu_buffer_rsrc_t rk = __builtin_amdgcn_make_buffer_rsrc((void*)(kc + kvbase), 0, 65536, 0x00020000);
    const __amdgpu_buffer_rsrc_t rv = __builtin_amdgcn_make_buffer_rsrc((void*)(vc + kvbase), 0, 65536, 0x00020000);
    v4u kf[2][4];
#pragma unroll
    for (int T = 0; T < 2; ++T)
#pragma unroll
        for (int ks = 0; ks < 4; ++ks)
            kf[T][ks] = __builtin_amdgcn_raw_buffer_load_b128(rk, (32 * wave + 16 * T + c16) * 256 + 64 * ks + 16 * qd, 0, AUX_NT);
    FENCE();

    v4u hn = {0u, 0u, 0u, 0u};
    if (wave < 4) hn = layernorm_pk(xp);

#pragma unroll
    for (int k = 0; k < 2; ++k) *(LAS v4u*)(L + L_WQ + (16 * wave + (lane >> 3) + 8 * k) * 144 + 16 * (lane & 7)) = wqs[k];
    FENCE();
    STAMP(20);
    v4u vr[8];
#pragma unroll
    for (int j = 0; j < 8; ++j) vr[j] = __builtin_amdgcn_raw_buffer_load_b128(rv, (32 * wave + 8 * qd + j) * 256 + 16 * c16, 0, AUX_NT);
    FENCE();
    __syncthreads();
    v4u wf[8];
    const int wv4 = wave & 3;
#pragma unroll
    for (int ks = 0; ks < 4; ++ks) {
        const LAS unsigned char* p = L + L_WQ + (32 * ks + 8 * qd + (c16 >> 2)) * 144 + (16 * wv4 + 4 * (c16 & 3)) * 2;
        const v2u lo = __builtin_bit_cast(v2u, __builtin_amdgcn_ds_read_tr16_b64_v4i16((LAS v4s*)p));
        const v2u hi = __builtin_bit_cast(v2u, __builtin_amdgcn_ds_read_tr16_b64_v4i16((LAS v4s*)(p + 4 * 144)));
        wf[ks] = v4u{lo.x, lo.y, hi.x, hi.y};
    }
    STAMP(1);

    const unsigned qp_off = WS_QP + (((g * 2 + (cb >> 2)) * 4 + rb) * 256 + (cb & 3) * 64 + 16 * wv4 + c16) * 4;
    auto stage_q = [&](v4u h, int layer) {
        const f32x4 z = {0.f, 0.f, 0.f, 0.f};
        const f32x4 d0 = mfma(h, wf[0], z), d1 = mfma(h, wf[1], z), d2 = mfma(h, wf[2], z), d3 = mfma(h, wf[3], z);
        const float q = (d0[0] + d1[1]) + (d2[2] + d3[3]);
        if (qd == rb) __builtin_amdgcn_raw_buffer_store_b32(canon(q), rs, qp_off + layer * QP_L, 0, AUX_SC1);
    };
    if (wave < 4) stage_q(hn, 0);
    STAMP(2);

    {
        f32x2 w1f[7], b1f[7];
#pragma unroll
        for (int k = 0; k < 7; ++k) { w1f[k] = f32x2{bflo(w1p[k]), bfhi(w1p[k])}; b1f[k] = f32x2{bflo(b1p[k]), bfhi(b1p[k])}; }
        float acc[4] = {0.f, 0.f, 0.f, 0.f};
#pragma unroll 1
        for (int u = 0; u < 4; ++u) {
            const unsigned idx = (unsigned)(16 * b + 4 * eg + u);
            const float tv = __uint_as_float((((idx >> 11) << 15) | ((idx & 0x7FFu) + (TAB_E0 << 7))) << 16);
            float a = 0.f;
#pragma unroll
            for (int k = 0; k < 7; ++k) a = mlp_pair(tv, w1f[k], b1f[k], w2p[k], a);
            acc[0] = acc[1]; acc[1] = acc[2]; acc[2] = acc[3]; acc[3] = a;
        }
        acc[0] = swap32_sum(acc[0], acc[2]); acc[1] = swap32_sum(acc[1], acc[3]);
        acc[0] = row16_sum(swap16_sum(acc[0], acc[1]));
        if (c16 == 0) *(LAS float*)(L + L_TL + (wave * 4 + qd) * 4) = acc[0];
    }
    FENCE();
    const __amdgpu_buffer_rsrc_t rwo = __builtin_amdgcn_make_buffer_rsrc((void*)(wo + (size_t)(512 * g + 256 * hp) * 512 + 32 * s), 0, 256 * 1024, 0x00020000);
    v4u wos[2];
#pragma unroll
    for (int k = 0; k < 2; ++k) wos[k] = __builtin_amdgcn_raw_buffer_load_b128(rwo, (32 * wave + 16 * k + (lane >> 2)) * 1024 + 16 * (lane & 3), 0, AUX_NT);
    FENCE();
    __syncthreads();
    if (tid < 16) {
        const int e2 = tid >> 2, u = tid & 3;
        const float sum = *(const LAS float*)(L + L_TL + ((2 * e2) * 4 + u) * 4) + *(const LAS float*)(L + L_TL + ((2 * e2 + 1) * 4 + u) * 4);
        unsigned y = cvtpk(rbf(sum) + b2f, 0.f) & 0xFFFFu;
        if (y == 0xFFFFu) y = 0x7FC0u;
        __builtin_amdgcn_raw_buffer_store_b16((unsigned short)y, rs, WS_TAB + (16 * b + tid) * 2, 0, AUX_SC1);
    }
    STAMP(3);

    v4u vf[8];
    const int qa0 = L_QS + (c16 == 0 ? 0 : c16 == 4 ? 256 : 512) + 16 * qd;
    const int qa1 = L_QS + (c16 == 8 ? 0 : c16 == 12 ? 256 : 512) + 16 * qd;

#pragma unroll
    for (int layer = 0; layer < 2; ++layer) {
        if (layer == 1) {
            if (wave == 0) gather_q(1);
            __syncthreads();
        }
        STAMP(4 + 8 * layer);
        {
            f32x4 d0 = {0.f, 0.f, 0.f, 0.f}, d1 = {0.f, 0.f, 0.f, 0.f};
#pragma unroll
            for (int ks = 0; ks < 4; ++ks) {
                d0 = mfma(*(const LAS v4u*)(L + qa0 + 64 * ks), kf[0][ks], d0);
                d1 = mfma(*(const LAS v4u*)(L + qa1 + 64 * ks), kf[1][ks], d1);
            }
            if (layer == 0) { asm volatile("" :: "v"(d0[0]), "v"(d1[0])); STAMP(21); }
            const float sc = rbf(rbf(d0[0] + d1[0]) * 0.08838834764831845f);
            const float m = swap32_max(row16_max(sc)) * 1.4426950408889634f;
            const float p = __builtin_amdgcn_exp2f(__builtin_fmaf(sc, 1.4426950408889634f, -m));
            const unsigned ph = cvtpk(p, p);
            const unsigned pl = cvtpk(p - bflo(ph), 0.f);
            LAS unsigned char* pw = L + L_PL + wave * 256 + ((qd & 1) * 32 + 16 * (qd >> 1) + c16) * 2;
            *(LAS unsigned short*)pw = (unsigned short)ph;
            *(LAS unsigned short*)(pw + 128) = (unsigned short)pl;
            float lsum = row16_sum(p);
            lsum = swap32_sum(lsum, lsum);
            if (layer == 0) {
#pragma unroll
                for (int t = 0; t < 8; ++t)
#pragma unroll
                    for (int jp = 0; jp < 4; ++jp)
                        vf[t][jp] = __builtin_amdgcn_perm(vr[2 * jp + 1][t >> 1], vr[2 * jp][t >> 1], (t & 1) ? 0x07060302u : 0x05040100u);
            }
            if (layer == 0) { asm volatile("" :: "v"(vf[0]), "v"(vf[7])); STAMP(22); }
            const v4u pf = *(const LAS v4u*)(L + L_PL + wave * 256 + (c16 & 3) * 64 + 16 * qd);
#pragma unroll
            for (int hf = 0; hf < 2; ++hf) {
                f32x4 o[4];
#pragma unroll
                for (int t = 0; t < 4; ++t) { f32x4 z = {0.f, 0.f, 0.f, 0.f}; o[t] = mfma(pf, vf[4 * hf + t], z); }
#pragma unroll
                for (int t = 0; t < 4; t += 2) {
                    f32x4 v = {o[t][0] + o[t][2], o[t][1] + o[t][3], o[t + 1][0] + o[t + 1][2], o[t + 1][1] + o[t + 1][3]};
                    if (lane < 16) *(LAS f32x4*)(L + L_OL + (wave * 256 + (8 * lane + 4 * hf + t) * 2) * 4) = v;
                }
                __builtin_amdgcn_sched_barrier(0);
            }
            if (c16 == 0 && qd < 2) *(LAS f32x2*)(L + L_ML + (qd * 8 + wave) * 8) = f32x2{m, lsum};
            STAMP(23 + layer);
        }
        __syncthreads();
        if (wave < 4) {
            const int h = wave >> 1, dim = tid & 127;
            const f32x2 ml = *(const LAS f32x2*)(L + L_ML + (h * 8 + (lane & 7)) * 8);
            float ov[8];
#pragma unroll
            for (int w = 0; w < 8; ++w) ov[w] = *(const LAS float*)(L + L_OL + (w * 256 + dim * 2 + h) * 4);
            const float M = row8_max(ml.x);
            const float f = __builtin_amdgcn_exp2f(ml.x - M);
            float o = 0.f;
#pragma unroll
            for (int w = 0; w < 8; ++w) o = __builtin_fmaf(__builtin_bit_cast(float, __builtin_amdgcn_readlane(__builtin_bit_cast(int, f), w)), ov[w], o);
            __builtin_amdgcn_raw_buffer_store_b32(canon(o), rs, WS_OP + layer * OP_L + (team * 16 + s) * 1024 + tid * 4, 0, AUX_SC1);
            float Ls = f * ml.y;
            Ls += dpp<ROR4>(Ls); Ls += dpp<ROR2>(Ls); Ls += dpp<ROR1>(Ls);
            if (dim == 0) __builtin_amdgcn_raw_buffer_store_b64(v2u{canon(M), canon(Ls)}, rs, WS_ML + layer * ML_L + (team * 16 + s) * 16 + h * 8, 0, AUX_SC1);
        }
        STAMP(5 + 8 * layer);
        if (layer == 0) {
#pragma unroll
            for (int k = 0; k < 2; ++k) *(LAS v4u*)(L + L_WO + (32 * wave + 16 * k + (lane >> 2)) * 80 + 16 * (lane & 3)) = wos[k];
            __syncthreads();
            if (wave == 4 || wave == 5) {
#pragma unroll
                for (int ks = 0; ks < 8; ++ks) {
                    const LAS unsigned char* p = L + L_WO + (32 * ks + 8 * qd + (c16 >> 2)) * 80 + (16 * (wave & 1) + 4 * (c16 & 3)) * 2;
                    const v2u lo = __builtin_bit_cast(v2u, __builtin_amdgcn_ds_read_tr16_b64_v4i16((LAS v4s*)p));
                    const v2u hi = __builtin_bit_cast(v2u, __builtin_amdgcn_ds_read_tr16_b64_v4i16((LAS v4s*)(p + 4 * 80)));
                    wf[ks] = v4u{lo.x, lo.y, hi.x, hi.y};
                }
            }
            v4u tv;
#pragma unroll 1
            for (unsigned sp = 0; sp < SPIN_CAP; ++sp) {
                FENCE();
                tv = __builtin_amdgcn_raw_buffer_load_b128(rs, WS_TAB + tid * 16, 0, AUX_SC1);
                const unsigned mx = max(umax4(tv), umax4(tv << 16));
                if (__all(mx < 0xFFFF0000u)) break;
                __builtin_amdgcn_s_sleep(2);
            }
            *(LAS v4u*)(L + L_TAB + tid * 16) = tv;
        } else {
            __builtin_amdgcn_s_sleep(D_E2);
        }
        {
            const unsigned ob = WS_OP + layer * OP_L + (team * 16 + c16) * 1024 + (32 * wave + 8 * qd) * 4;
            const unsigned mb = WS_ML + layer * ML_L + (team * 16 + c16) * 16 + (wave >> 2) * 8;
            v4u o0, o1; v2u ml;
#pragma unroll 1
            for (unsigned sp = 0; sp < SPIN_CAP; ++sp) {
                FENCE();
                o0 = __builtin_amdgcn_raw_buffer_load_b128(rs, ob, 0, AUX_SC1);
                o1 = __builtin_amdgcn_raw_buffer_load_b128(rs, ob + 16, 0, AUX_SC1);
                ml = __builtin_amdgcn_raw_buffer_load_b64(rs, mb, 0, AUX_SC1);
                const unsigned mx = max(umax3(umax4(o0), umax4(o1), ml.x), ml.y);
                if (__all(mx != SENT)) break;
                __builtin_amdgcn_s_sleep(2);
            }
            STAMP(6 + 8 * layer);
            const float m = __uint_as_float(ml.x);
            const float f = __builtin_amdgcn_exp2f(m - row16_max(m));
            const float Ls = row16_sum(f * __uint_as_float(ml.y));
            const float sc = f * __builtin_amdgcn_rcpf(Ls);
            f32x4 a = __builtin_bit_cast(f32x4, o0) * sc, c = __builtin_bit_cast(f32x4, o1) * sc;
            const bool b8 = c16 & 8, b4 = c16 & 4, b2 = c16 & 2;
            float v[4];
#pragma unroll
            for (int j = 0; j < 4; ++j) { const float keep = b8 ? c[j] : a[j], send = b8 ? a[j] : c[j]; v[j] = keep + dpp<ROR8>(send); }
            float w0, w1v;
            { const float k0 = b4 ? v[2] : v[0], s0 = b4 ? v[0] : v[2], k1 = b4 ? v[3] : v[1], s1 = b4 ? v[1] : v[3];
              w0 = k0 + dpp<0x141>(s0); w1v = k1 + dpp<0x141>(s1); }
            float r;
            { const float k0 = b2 ? w1v : w0, s0 = b2 ? w0 : w1v; r = k0 + dpp<0x4E>(s0); }
            r += dpp<0xB1>(r);
            if ((c16 & 1) == 0) *(LAS unsigned short*)(L + L_OB + (32 * wave + 8 * qd + (c16 >> 1)) * 2) = (unsigned short)cvtpk(r, r);
        }
        __syncthreads();
        if (wave == 4 || wave == 5) {
            f32x4 d = {0.f, 0.f, 0.f, 0.f};
#pragma unroll
            for (int ks = 0; ks < 8; ++ks) d = mfma(*(const LAS v4u*)(L + L_OB + 64 * ks + 16 * qd), wf[ks], d);
            if (lane < 16) __builtin_amdgcn_raw_buffer_store_b32(canon(d[0]), rs, WS_XP + layer * XP_L + (team * 512 + 32 * s + 16 * (wave & 1) + lane) * 4, 0, AUX_SC1);
        }
        STAMP(7 + 8 * layer);
        if (layer == 1 && b != 0) break;
        __builtin_amdgcn_s_sleep(D_E3);
        {
            const unsigned xb = WS_XP + layer * XP_L + (4 * qd * 512 + 64 * wave + 4 * c16) * 4;
            v4u xv[4];
#pragma unroll 1
            for (unsigned sp = 0; sp < SPIN_CAP; ++sp) {
                FENCE();
#pragma unroll
                for (int k = 0; k < 4; ++k) xv[k] = __builtin_amdgcn_raw_buffer_load_b128(rs, xb + k * 2048, 0, AUX_SC1);
                const unsigned mx = max(max(umax4(xv[0]), umax4(xv[1])), max(umax4(xv[2]), umax4(xv[3])));
                if (__all(mx != SENT)) break;
                __builtin_amdgcn_s_sleep(2);
            }
            const f32x4 v = (__builtin_bit_cast(f32x4, xv[0]) + __builtin_bit_cast(f32x4, xv[1])) + (__builtin_bit_cast(f32x4, xv[2]) + __builtin_bit_cast(f32x4, xv[3]));
            const float a0 = swap32_sum(v[0], v[2]), a1 = swap32_sum(v[1], v[3]);
            const float a = swap16_sum(a0, a1);
            *(LAS float*)(L + L_AL + (64 * wave + 4 * c16 + qd) * 4) = rbf(a);
        }
        __syncthreads();
        STAMP(8 + 8 * layer);
        if (wave < 4) {
            const f32x4 a0 = *(const LAS f32x4*)(L + L_AL + 32 * lg), a1 = *(const LAS f32x4*)(L + L_AL + 32 * lg + 16);
            { f32x2 t = unpk(xp.x) + f32x2{a0[0], a0[1]}; xp.x = cvtpk(t.x, t.y); }
            { f32x2 t = unpk(xp.y) + f32x2{a0[2], a0[3]}; xp.y = cvtpk(t.x, t.y); }
            { f32x2 t = unpk(xp.z) + f32x2{a1[0], a1[1]}; xp.z = cvtpk(t.x, t.y); }
            { f32x2 t = unpk(xp.w) + f32x2{a1[2], a1[3]}; xp.w = cvtpk(t.x, t.y); }
            const v4u h2 = layernorm_pk(layernorm_pk(xp));
            unsigned yv[8], big = 0;
#pragma unroll
            for (int k = 0; k < 8; ++k) {
                const unsigned u = (k & 1) ? (h2[k >> 1] >> 16) : (h2[k >> 1] & 0xFFFFu);
                const unsigned rel = (u & 0x7FFFu) - (unsigned)(TAB_E0 << 7);
                big = max(big, rel);
                const unsigned idx = ((u >> 15) << 11) | (rel & 2047u);
                yv[k] = *(const LAS unsigned short*)(L + L_TAB + idx * 2);
            }
            if (__any(big >= 2048u)) {
#pragma unroll 1
                for (int k = 0; k < 8; ++k) {
                    unsigned hw = h2[0];
#pragma unroll
                    for (int kk = 1; kk < 4; ++kk) hw = ((k >> 1) == kk) ? h2[kk] : hw;
                    const unsigned u = (k & 1) ? (hw >> 16) : (hw & 0xFFFFu);
                    unsigned long long mk = __ballot(((u & 0x7FFFu) - (unsigned)(TAB_E0 << 7)) >= 2048u);
#pragma unroll 1
                    while (mk) {
                        const int ln = __builtin_ctzll(mk);
                        mk &= mk - 1;
                        const float tv = __uint_as_float((unsigned)__builtin_amdgcn_readlane((int)u, ln) << 16);
                        float a = 0.f;
#pragma unroll 1
                        for (int p = lane; p < FF / 2; p += 64) {
                            const unsigned ww1 = *(const unsigned*)(w1 + 2 * p), bb1 = *(const unsigned*)(b1 + 2 * p), ww2 = *(const unsigned*)(w2 + 2 * p);
                            a = mlp_pair(tv, f32x2{bflo(ww1), bfhi(ww1)}, f32x2{bflo(bb1), bfhi(bb1)}, ww2, a);
                        }
                        const unsigned y = cvtpk(rbf(wave_sum(a)) + b2f, 0.f) & 0xFFFFu;
#pragma unroll
                        for (int kk = 0; kk < 8; ++kk) yv[kk] = (k == kk && lane == ln) ? y : yv[kk];
                    }
                }
            }
#pragma unroll
            for (int k = 0; k < 4; ++k) {
                const f32x2 t = unpk(xp[k]) + f32x2{__uint_as_float(yv[2 * k] << 16), __uint_as_float(yv[2 * k + 1] << 16)};
                xp[k] = cvtpk(t.x, t.y);
            }
            if (layer == 0) {
                stage_q(layernorm_pk(xp), 1);
                STAMP(9);
                __builtin_amdgcn_s_sleep(D_E1);
            } else if (wave == 0) {
                *(v4u*)(out + 8 * lg) = xp;
            }
        }
    }
    STAMP(17);
    STAMP_DUMP();
}

extern "C" void kernel_launch(void* const* d_in, const int* in_sizes, int n_in, void* d_out, int out_size, void* d_ws,
                              size_t ws_size, hipStream_t stream) {
    const bf16_t* x = (const bf16_t*)d_in[0];
    const bf16_t* kc = (const bf16_t*)d_in[1];
    const bf16_t* vc = (const bf16_t*)d_in[2];
    const bf16_t* wq = (const bf16_t*)d_in[3];
    const bf16_t* wo = (const bf16_t*)d_in[4];
    const bf16_t* w1 = (const bf16_t*)d_in[6];
    const bf16_t* b1 = (const bf16_t*)d_in[7];
    const bf16_t* w2 = (const bf16_t*)d_in[8];
    const bf16_t* b2 = (const bf16_t*)d_in[9];
    (void)hipFuncSetAttribute((const void*)fused_kernel, hipFuncAttributeMaxDynamicSharedMemorySize, LDS_BYTES);
    fused_kernel<<<NWG, NTHR, LDS_BYTES, stream>>>(x, kc, vc, wq, wo, w1, b1, w2, b2, (bf16_t*)d_out, d_ws);
}
```

```cpp
#include <hip/hip_runtime.h>
#include <hip/hip_bf16.h>
#include <stdint.h>

typedef unsigned short bf16_t;
typedef unsigned v4u __attribute__((ext_vector_type(4)));
typedef unsigned v2u __attribute__((ext_vector_type(2)));
typedef float f32x4 __attribute__((ext_vector_type(4)));
typedef float f32x2 __attribute__((ext_vector_type(2)));
typedef short bf16x8 __attribute__((ext_vector_type(8)));
typedef short v4s __attribute__((ext_vector_type(4)));
typedef __bf16 bf16x2 __attribute__((ext_vector_type(2)));
#define LAS __attribute__((address_space(3)))

#define D_E1 6
#define D_E2 6
#define D_E3 12
#define D_K0 12
#define D_FL 6
#define D_Q0 8
#define TABK 7
#define KB1 31
#define AUX_MW 0
#define INFL 32
#define D_E2A 2
#define D_E2X 6
#define D_E3A 4

constexpr int DM = 512, SK = 65536, WIN = 4096, FF = 1792;
constexpr int NWG = 256, NTHR = 640;
constexpr unsigned SENT = 0xFFFFFFFFu;
constexpr int AUX_SC1 = 16, AUX_NT = 2;
constexpr unsigned SPIN_CAP = 1u << 20;

constexpr unsigned WS_EPOCH = 0;
constexpr unsigned WS_FLAGS = 4096;
constexpr unsigned WS_TAB = 8192;
constexpr unsigned WS_QP = 16384;
constexpr unsigned QP_L = 8 * 4 * 512 * 4;
constexpr unsigned WS_OP = WS_QP + 2 * QP_L;
constexpr unsigned OP_L = 8 * 2 * 32 * 256 * 4;
constexpr unsigned WS_ML = WS_OP + 2 * OP_L;
constexpr unsigned ML_L = 8 * 2 * 32 * 4 * 4;
constexpr unsigned WS_XP = WS_ML + 2 * ML_L;
constexpr unsigned XP_L = 16 * 512 * 4;
constexpr unsigned WS_END = WS_XP + 2 * XP_L;
constexpr unsigned WS_STAMP = 4u << 20;

constexpr int L_K = 0;
constexpr int L_V = 32768;
constexpr int L_WQ = 65536;
constexpr int L_WO = L_WQ + 16384;
constexpr int L_MW = L_WO + 256 * 80;
constexpr int L_TAB = L_MW + 3 * 3584;
constexpr int L_OL = L_TAB + 8192;
constexpr int L_PL = L_OL + 8192;
constexpr int L_AL = L_PL + 2048;
constexpr int L_OB = L_AL + 2048;
constexpr int L_ML = L_OB + 512;
constexpr int L_QS = L_ML + 128;
constexpr int L_TL = L_QS + 1280;
constexpr int L_END = L_TL + 128;
constexpr int LDS_BYTES = 144 * 1024;
static_assert(L_END + 2560 <= LDS_BYTES, "lds");
static_assert(L_QS % 16 == 0 && L_MW % 16 == 0 && L_TL % 16 == 0 && L_TAB % 16 == 0 && L_OL % 16 == 0, "align");

constexpr int TAB_E0 = 114;

#define FENCE() asm volatile("" ::: "memory")

__device__ __forceinline__ float bflo(unsigned p) { return __uint_as_float(p << 16); }
__device__ __forceinline__ float bfhi(unsigned p) { return __uint_as_float(p & 0xFFFF0000u); }
__device__ __forceinline__ unsigned cvtpk(float a, float b) {
    f32x2 v = {a, b};
    return __builtin_bit_cast(unsigned, __builtin_convertvector(v, bf16x2));
}
__device__ __forceinline__ float rbf(float a) { return bflo(cvtpk(a, a)); }
__device__ __forceinline__ float dot2(unsigned a, unsigned b, float c) {
    return __builtin_amdgcn_fdot2_f32_bf16(__builtin_bit_cast(bf16x2, a), __builtin_bit_cast(bf16x2, b), c, false);
}
template <int CTRL> __device__ __forceinline__ float dpp(float x) {
    return __builtin_bit_cast(float, __builtin_amdgcn_mov_dpp(__builtin_bit_cast(int, x), CTRL, 0xf, 0xf, true));
}
constexpr int ROR1 = 0x121, ROR2 = 0x122, ROR4 = 0x124, ROR8 = 0x128;
__device__ __forceinline__ float swap32_sum(float a, float b) {
    auto r = __builtin_amdgcn_permlane32_swap(__float_as_uint(a), __float_as_uint(b), false, false);
    return __uint_as_float(r[0]) + __uint_as_float(r[1]);
}
__device__ __forceinline__ float swap16_sum(float a, float b) {
    auto r = __builtin_amdgcn_permlane16_swap(__float_as_uint(a), __float_as_uint(b), false, false);
    return __uint_as_float(r[0]) + __uint_as_float(r[1]);
}
__device__ __forceinline__ float swap32_max(float a) {
    auto r = __builtin_amdgcn_permlane32_swap(__float_as_uint(a), __float_as_uint(a), false, false);
    float o;
    asm("v_max_f32 %0, %1, %2" : "=v"(o) : "v"(r[0]), "v"(r[1]));
    return o;
}
__device__ __forceinline__ float swap16_max(float a) {
    auto r = __builtin_amdgcn_permlane16_swap(__float_as_uint(a), __float_as_uint(a), false, false);
    float o;
    asm("v_max_f32 %0, %1, %2" : "=v"(o) : "v"(r[0]), "v"(r[1]));
    return o;
}
__device__ __forceinline__ float row16_sum(float v) {
    v += dpp<ROR8>(v); v += dpp<ROR4>(v); v += dpp<ROR2>(v); v += dpp<ROR1>(v);
    return v;
}
#define MAX_DPP(v, CTRL) asm volatile("s_nop 1\n\tv_max_f32_dpp %0, %0, %0 " CTRL " row_mask:0xf bank_mask:0xf" : "+v"(v))
__device__ __forceinline__ float row16_max(float v) {
    MAX_DPP(v, "row_ror:8"); MAX_DPP(v, "row_ror:4"); MAX_DPP(v, "row_ror:2"); MAX_DPP(v, "row_ror:1");
    return v;
}
__device__ __forceinline__ float row8_max(float v) {
    MAX_DPP(v, "row_ror:4"); MAX_DPP(v, "row_ror:2"); MAX_DPP(v, "row_ror:1");
    return v;
}
__device__ __forceinline__ float wave_sum(float v) { v = row16_sum(v); v = swap16_sum(v, v); return swap32_sum(v, v); }
__device__ __forceinline__ unsigned canon(float v) {
    unsigned u = __float_as_uint(v);
    return u < SENT ? u : 0x7FC00000u;
}
__device__ __forceinline__ unsigned umax3(unsigned a, unsigned b, unsigned c) { return max(max(a, b), c); }
__device__ __forceinline__ unsigned umax4(v4u a) { return max(umax3(a.x, a.y, a.z), a.w); }
__device__ __forceinline__ f32x4 mfma(v4u a, v4u b, f32x4 c) {
    return __builtin_amdgcn_mfma_f32_16x16x32_bf16(__builtin_bit_cast(bf16x8, a), __builtin_bit_cast(bf16x8, b), c, 0, 0, 0);
}
__device__ __forceinline__ float mlp_pair(float t, f32x2 w1, f32x2 b1, unsigned w2p, float acc) {
    const f32x2 c1 = {-2.3022081986f, -2.3022081986f}, c2 = {-0.1029432396f, -0.1029432396f};
    const f32x2 tt = {t, t};
    const f32x2 a = tt * w1;
    const unsigned p = cvtpk(a.x, a.y);
    f32x2 u = {bflo(p), bfhi(p)};
    u += b1;
    const unsigned q = cvtpk(u.x, u.y);
    u.x = bflo(q); u.y = bfhi(q);
    const f32x2 z = u * __builtin_elementwise_fma(u * u, c2, c1);
    f32x2 e = {__builtin_amdgcn_exp2f(z.x), __builtin_amdgcn_exp2f(z.y)};
    e += 1.0f;
    const f32x2 r = {__builtin_amdgcn_rcpf(e.x), __builtin_amdgcn_rcpf(e.y)};
    const f32x2 gl = u * r;
    return dot2(cvtpk(gl.x, gl.y), w2p, acc);
}

__device__ __forceinline__ void mlp_pair2(float t0, float t1, f32x2 w1, f32x2 b1, unsigned w2p, float& acc0, float& acc1) {
    const f32x2 c1 = {-2.3022081986f, -2.3022081986f}, c2 = {-0.1029432396f, -0.1029432396f};
    const f32x2 a0 = f32x2{t0, t0} * w1, a1 = f32x2{t1, t1} * w1;
    const unsigned p0 = cvtpk(a0.x, a0.y), p1 = cvtpk(a1.x, a1.y);
    f32x2 u0 = {bflo(p0), bfhi(p0)}, u1 = {bflo(p1), bfhi(p1)};
    u0 += b1; u1 += b1;
    const unsigned q0 = cvtpk(u0.x, u0.y), q1 = cvtpk(u1.x, u1.y);
    u0 = f32x2{bflo(q0), bfhi(q0)}; u1 = f32x2{bflo(q1), bfhi(q1)};
    const f32x2 s0 = u0 * u0, s1 = u1 * u1;
    const f32x2 y0 = __builtin_elementwise_fma(s0, c2, c1), y1 = __builtin_elementwise_fma(s1, c2, c1);
    const f32x2 z0 = u0 * y0, z1 = u1 * y1;
    f32x2 e0 = {__builtin_amdgcn_exp2f(z0.x), __builtin_amdgcn_exp2f(z0.y)}, e1 = {__builtin_amdgcn_exp2f(z1.x), __builtin_amdgcn_exp2f(z1.y)};
    e0 += 1.0f; e1 += 1.0f;
    const f32x2 r0 = {__builtin_amdgcn_rcpf(e0.x), __builtin_amdgcn_rcpf(e0.y)}, r1 = {__builtin_amdgcn_rcpf(e1.x), __builtin_amdgcn_rcpf(e1.y)};
    const f32x2 g0 = u0 * r0, g1 = u1 * r1;
    const unsigned h0 = cvtpk(g0.x, g0.y), h1 = cvtpk(g1.x, g1.y);
    acc0 = dot2(h0, w2p, acc0); acc1 = dot2(h1, w2p, acc1);
}
__device__ __forceinline__ f32x2 unpk(unsigned p) { return f32x2{bflo(p), bfhi(p)}; }
__device__ __forceinline__ v4u layernorm_pk(v4u xp) {
    f32x2 d[4];
#pragma unroll
    for (int k = 0; k < 4; ++k) d[k] = unpk(xp[k]);
    const f32x2 s = (d[0] + d[1]) + (d[2] + d[3]);
    const float mean = wave_sum(s.x + s.y) * (1.0f / DM);
    const float m = rbf(mean);
    const f32x2 mm = {m, m};
#pragma unroll
    for (int k = 0; k < 4; ++k) d[k] -= mm;
    f32x2 q = d[0] * d[0];
#pragma unroll
    for (int k = 1; k < 4; ++k) q = __builtin_elementwise_fma(d[k], d[k], q);
    const float dm = mean - m;
    const float var = fmaxf(__builtin_fmaf(-dm, dm, wave_sum(q.x + q.y) * (1.0f / DM)), 0.f);
    const float sd = rbf(__builtin_amdgcn_sqrtf(rbf(rbf(var) + 1e-5f)));
    const float r = __builtin_amdgcn_rcpf(sd);
    const f32x2 rr = {r, r};
    v4u o;
#pragma unroll
    for (int k = 0; k < 4; ++k) {
        const f32x2 t = unpk(cvtpk(d[k].x, d[k].y)) * rr;
        o[k] = cvtpk(t.x, t.y);
    }
    return o;
}

#define STAMP(n) do { } while (0)
#define STAMP_DUMP() do { } while (0)
#define WAITVM(n) do { } while (0)

__global__ void __launch_bounds__(NTHR, 3)
fused_kernel(const bf16_t* x, const bf16_t* kc, const bf16_t* vc, const bf16_t* wq, const bf16_t* wo, const bf16_t* w1,
             const bf16_t* b1, const bf16_t* w2, const bf16_t* b2, bf16_t* out, void* ws) {
    extern __shared__ __attribute__((aligned(16))) unsigned char smem[];
    LAS unsigned char* L = (LAS unsigned char*)smem;
    const int b = blockIdx.x, tid = threadIdx.x, lane = tid & 63;
    const int wave = __builtin_amdgcn_readfirstlane(tid >> 6);
    const int g = b & 7, i = b >> 3, hp = i & 1, j = i >> 1, cb = i & 7, rb = i >> 3;
    const __amdgpu_buffer_rsrc_t rs = __builtin_amdgcn_make_buffer_rsrc(ws, 0, (int)WS_END, 0x00020000);
    const unsigned team = (unsigned)(g * 2 + hp);
    STAMP(0);

    auto gather_q = [&](int layer) {
        const unsigned base = WS_QP + layer * QP_L + g * 8192 + lane * 16;
        v4u qv[8];
#pragma unroll 1
        for (unsigned sp = 0; sp < SPIN_CAP; ++sp) {
            FENCE();
#pragma unroll
            for (int k = 0; k < 8; ++k) qv[k] = __builtin_amdgcn_raw_buffer_load_b128(rs, base + k * 1024, 0, AUX_SC1);
            unsigned mx = 0;
#pragma unroll
            for (int k = 0; k < 8; ++k) mx = max(mx, umax4(qv[k]));
            if (__all(mx != SENT)) break;
            __builtin_amdgcn_s_sleep(2);
        }
#pragma unroll
        for (int hf = 0; hf < 2; ++hf) {
            const f32x4 q = (__builtin_bit_cast(f32x4, qv[hf]) + __builtin_bit_cast(f32x4, qv[2 + hf])) + (__builtin_bit_cast(f32x4, qv[4 + hf]) + __builtin_bit_cast(f32x4, qv[6 + hf]));
            *(LAS v2u*)(L + L_QS + hf * 512 + lane * 8) = v2u{cvtpk(q[0], q[1]), cvtpk(q[2], q[3])};
        }
    };

    if (wave == 9) {
        __builtin_amdgcn_s_setprio(3);
        const size_t kvb = ((size_t)g * SK + (SK - WIN) + 128 * i) * 128;
        const __amdgpu_buffer_rsrc_t rk = __builtin_amdgcn_make_buffer_rsrc((void*)(kc + kvb), 0, 32768, 0x00020000);
        const __amdgpu_buffer_rsrc_t rv = __builtin_amdgcn_make_buffer_rsrc((void*)(vc + kvb), 0, 32768, 0x00020000);
        int ko[4], vo[4];
#pragma unroll
        for (int k = 0; k < 4; ++k) {
            const int row = 4 * k + (lane >> 4);
            ko[k] = row * 256 + 16 * ((lane & 15) ^ row);
            vo[k] = row * 256 + 16 * ((lane & 15) ^ (2 * ((row & 3) | ((row >> 3) << 2))));
        }
        __builtin_amdgcn_s_barrier();
        __builtin_amdgcn_s_sleep(D_K0);
#pragma unroll
        for (int it = 0; it < 64; ++it) {
            if (it < 32) __builtin_amdgcn_raw_ptr_buffer_load_lds(rk, (LAS unsigned*)(L + L_K + it * 1024), 16, ko[it & 3], (it >> 2) * 4096, 0, AUX_NT);
            else __builtin_amdgcn_raw_ptr_buffer_load_lds(rv, (LAS unsigned*)(L + L_V + (it - 32) * 1024), 16, vo[it & 3], ((it - 32) >> 2) * 4096, 0, AUX_NT);
            if (it == KB1) __builtin_amdgcn_s_barrier();
        }
        asm volatile("s_waitcnt vmcnt(0)" ::: "memory");
        STAMP(20);
        __builtin_amdgcn_s_barrier();
        STAMP_DUMP();
        return;
    }
    if (wave == 8) {
        __builtin_amdgcn_s_setprio(3);
        const v2u ep = __builtin_amdgcn_raw_buffer_load_b64(rs, WS_EPOCH, 0, AUX_SC1);
        const v4u sv = {SENT, SENT, SENT, SENT};
#pragma unroll
        for (int l2 = 0; l2 < 2; ++l2) {
            __builtin_amdgcn_raw_buffer_store_b128(sv, rs, WS_OP + l2 * OP_L + ((g * 2) * 32 + i) * 1024 + lane * 16, 0, AUX_SC1);
            __builtin_amdgcn_raw_buffer_store_b128(sv, rs, WS_OP + l2 * OP_L + ((g * 2 + 1) * 32 + i) * 1024 + lane * 16, 0, AUX_SC1);
            unsigned off;
            if (lane < 16) off = WS_QP + l2 * QP_L + ((g * 4 + rb) * 512 + cb * 64) * 4 + lane * 16;
            else if (lane < 24) off = WS_XP + l2 * XP_L + (team * 512 + j * 32) * 4 + (lane - 16) * 16;
            else if (lane < 26) off = WS_ML + l2 * ML_L + ((g * 2 + (lane - 24)) * 32 + i) * 16;
            else off = WS_TAB + b * 32 + (lane & 1) * 16;
            if (lane < 28) __builtin_amdgcn_raw_buffer_store_b128(sv, rs, off, 0, AUX_SC1);
        }
        *(LAS unsigned*)(L + L_QS + 1024 + lane * 4) = 0u;
        FENCE();
        __builtin_amdgcn_s_barrier();
        FENCE();
        const unsigned long long T = (((unsigned long long)ep.y << 32) | ep.x) + 0x9E3779B97F4A7C15ull;
        const unsigned tlo = __builtin_amdgcn_readfirstlane((unsigned)T), thi = __builtin_amdgcn_readfirstlane((unsigned)(T >> 32));
        asm volatile("s_waitcnt vmcnt(0)" ::: "memory");
        STAMP(20);
        if (lane == 0) __builtin_amdgcn_raw_buffer_store_b64(v2u{tlo, thi}, rs, WS_FLAGS + b * 8, 0, AUX_SC1);
        __syncthreads();
        __builtin_amdgcn_s_sleep(D_FL);
#pragma unroll 1
        for (unsigned sp = 0; sp < SPIN_CAP; ++sp) {
            FENCE();
            const v4u f0 = __builtin_amdgcn_raw_buffer_load_b128(rs, WS_FLAGS + lane * 32, 0, AUX_SC1);
            const v4u f1 = __builtin_amdgcn_raw_buffer_load_b128(rs, WS_FLAGS + lane * 32 + 16, 0, AUX_SC1);
            const unsigned bad = (f0.x ^ tlo) | (f0.y ^ thi) | (f0.z ^ tlo) | (f0.w ^ thi) | (f1.x ^ tlo) | (f1.y ^ thi) | (f1.z ^ tlo) | (f1.w ^ thi);
            if (__all(bad == 0u)) break;
            __builtin_amdgcn_s_sleep(16);
        }
        if (b == 0 && lane == 0) __builtin_amdgcn_raw_buffer_store_b64(v2u{tlo, thi}, rs, WS_EPOCH, 0, AUX_SC1);
        STAMP(21);
        __builtin_amdgcn_s_sleep(D_Q0);
        gather_q(0);
        STAMP(22);
        __syncthreads();
        STAMP_DUMP();
        return;
    }

    const int qd = lane >> 4, c16 = lane & 15, wv4 = wave & 3;
    const int lg = 16 * (c16 >> 2) + 4 * (c16 & 3) + qd;
    v4u xp = {0u, 0u, 0u, 0u};
    if (wave < 4) xp = *(const v4u*)(x + 8 * lg);
    const __amdgpu_buffer_rsrc_t rwq = __builtin_amdgcn_make_buffer_rsrc((void*)(wq + (size_t)(128 * rb) * 4096 + 512 * g + 64 * cb), 0, 128 * 8192, 0x00020000);
    v4u st0, st1;
    st0 = __builtin_amdgcn_raw_buffer_load_b128(rwq, (16 * wave + (lane >> 3)) * 8192 + 16 * (lane & 7), 0, AUX_NT);
    st1 = __builtin_amdgcn_raw_buffer_load_b128(rwq, (16 * wave + (lane >> 3) + 8) * 8192 + 16 * (lane & 7), 0, AUX_NT);
    FENCE();
    const int pg = tid & 127, eg = tid >> 7;
    v4u mw0 = {0u, 0u, 0u, 0u}, mw1 = {0u, 0u, 0u, 0u};
    if ((tid & 255) < 224) {
        mw0 = *(const v4u*)((wave < 4 ? w1 : b1) + 8 * (tid & 255));
        if (wave < 4) mw1 = *(const v4u*)(w2 + 8 * tid);
    }
    const float b2f = __uint_as_float((unsigned)b2[0] << 16);
    FENCE();
    __builtin_amdgcn_s_barrier();
    FENCE();

    v4u hn = {0u, 0u, 0u, 0u};
    if (wave < 4) hn = layernorm_pk(xp);
    *(LAS v4u*)(L + L_WQ + (16 * wave + (lane >> 3)) * 128 + 16 * (lane & 7)) = st0;
    *(LAS v4u*)(L + L_WQ + (16 * wave + (lane >> 3) + 8) * 128 + 16 * (lane & 7)) = st1;
    if ((tid & 255) < 224) {
        *(LAS v4u*)(L + L_MW + (wave < 4 ? 0 : 3584) + 16 * (tid & 255)) = mw0;
        if (wave < 4) *(LAS v4u*)(L + L_MW + 7168 + 16 * tid) = mw1;
    }
    STAMP(20);
    __syncthreads();
    v4u fr[16];
    const unsigned qp_off = WS_QP + ((g * 4 + rb) * 512 + cb * 64 + 16 * wv4 + c16) * 4;
    auto stage_q = [&](v4u h, int layer) {
        const f32x4 z = {0.f, 0.f, 0.f, 0.f};
        const f32x4 d0 = mfma(h, fr[0], z), d1 = mfma(h, fr[1], z), d2 = mfma(h, fr[2], z), d3 = mfma(h, fr[3], z);
        const float q = (d0[0] + d1[1]) + (d2[2] + d3[3]);
        if (qd == rb) __builtin_amdgcn_raw_buffer_store_b32(canon(q), rs, qp_off + layer * QP_L, 0, AUX_SC1);
    };
    if (wave < 4) {
#pragma unroll
        for (int ks = 0; ks < 4; ++ks) {
            const LAS unsigned char* p = L + L_WQ + (32 * ks + 8 * qd + (c16 >> 2)) * 128 + (16 * wv4 + 4 * (c16 & 3)) * 2;
            const v2u lo = __builtin_bit_cast(v2u, __builtin_amdgcn_ds_read_tr16_b64_v4i16((LAS v4s*)p));
            const v2u hi = __builtin_bit_cast(v2u, __builtin_amdgcn_ds_read_tr16_b64_v4i16((LAS v4s*)(p + 4 * 128)));
            fr[ks] = v4u{lo.x, lo.y, hi.x, hi.y};
        }
        stage_q(hn, 0);
    }
    STAMP(2);

    {
        f32x2 w1f[7], b1f[7];
        unsigned w2p[7];
#pragma unroll
        for (int k = 0; k < 7; ++k) {
            w1f[k] = unpk(*(const LAS unsigned*)(L + L_MW + 4 * (pg + 128 * k)));
            b1f[k] = unpk(*(const LAS unsigned*)(L + L_MW + 3584 + 4 * (pg + 128 * k)));
            w2p[k] = *(const LAS unsigned*)(L + L_MW + 7168 + 4 * (pg + 128 * k));
        }
        float acc[4] = {0.f, 0.f, 0.f, 0.f};
#pragma unroll
        for (int u = 0; u < 4; u += 2) {
            const unsigned idx = (unsigned)(16 * b + 4 * eg + u);
            const float tv0 = __uint_as_float((((idx >> 11) << 15) | ((idx & 0x7FFu) + (TAB_E0 << 7))) << 16);
            const float tv1 = __uint_as_float(__float_as_uint(tv0) + 0x10000u);
#pragma unroll
            for (int k = 0; k < TABK; ++k) {
                mlp_pair2(tv0, tv1, w1f[k], b1f[k], w2p[k], acc[u], acc[u + 1]);
                __builtin_amdgcn_sched_barrier(0);
            }
        }
        acc[0] = swap32_sum(acc[0], acc[2]); acc[1] = swap32_sum(acc[1], acc[3]);
        acc[0] = row16_sum(swap16_sum(acc[0], acc[1]));
        if (c16 == 0) *(LAS float*)(L + L_TL + (wave * 4 + qd) * 4) = acc[0];
    }
    STAMP(25);
    FENCE();
    const __amdgpu_buffer_rsrc_t rwo = __builtin_amdgcn_make_buffer_rsrc((void*)(wo + (size_t)(512 * g + 256 * hp) * 512 + 32 * j), 0, 256 * 1024, 0x00020000);
    st0 = __builtin_amdgcn_raw_buffer_load_b128(rwo, (32 * wave + (lane >> 2)) * 1024 + 16 * (lane & 3), 0, AUX_NT);
    st1 = __builtin_amdgcn_raw_buffer_load_b128(rwo, (32 * wave + 16 + (lane >> 2)) * 1024 + 16 * (lane & 3), 0, AUX_NT);
    FENCE();
    __syncthreads();
    STAMP(3);
    if (wave >= 4) {
#pragma unroll
        for (int T = 0; T < 2; ++T)
#pragma unroll
            for (int ks = 0; ks < 4; ++ks)
                fr[4 * T + ks] = *(const LAS v4u*)(L + L_K + (32 * wv4 + 16 * T + c16) * 256 + 16 * ((4 * ks + qd) ^ c16));
        const int vrow = 32 * wv4 + 8 * qd + (c16 >> 2);
        const int vsw = 2 * ((vrow & 3) | (((vrow >> 3) & 1) << 2));
        const LAS unsigned char* vb = L + L_V + vrow * 256 + 8 * (c16 & 1);
#pragma unroll
        for (int t = 0; t < 8; ++t) {
            const LAS unsigned char* p = vb + 16 * ((2 * t + ((c16 >> 1) & 1)) ^ vsw);
            const v2u lo = __builtin_bit_cast(v2u, __builtin_amdgcn_ds_read_tr16_b64_v4i16((LAS v4s*)p));
            const v2u hi = __builtin_bit_cast(v2u, __builtin_amdgcn_ds_read_tr16_b64_v4i16((LAS v4s*)(p + 4 * 256)));
            fr[8 + t] = v4u{lo.x, lo.y, hi.x, hi.y};
        }
    } else if (tid < 16) {
        const int e2 = tid >> 2, u = tid & 3;
        const float sum = *(const LAS float*)(L + L_TL + ((2 * e2) * 4 + u) * 4) + *(const LAS float*)(L + L_TL + ((2 * e2 + 1) * 4 + u) * 4);
        unsigned y = cvtpk(rbf(sum) + b2f, 0.f) & 0xFFFFu;
        if (y == 0xFFFFu) y = 0x7FC0u;
        __builtin_amdgcn_raw_buffer_store_b16((unsigned short)y, rs, WS_TAB + (16 * b + tid) * 2, 0, AUX_SC1);
    }
    const int qa0 = L_QS + ((c16 & 3) == 0 ? (c16 >> 2) * 256 : 1024) + 16 * qd;
    const int qa1 = L_QS + ((c16 & 3) == 1 ? (c16 >> 2) * 256 : 1024) + 16 * qd;

#pragma unroll
    for (int layer = 0; layer < 2; ++layer) {
        if (layer == 1) {
            if (wave == 0) { __builtin_amdgcn_s_sleep(D_E1); gather_q(1); }
            __syncthreads();
        }
        STAMP(4 + 8 * layer);
        if (wave >= 4) {
            f32x4 d = {0.f, 0.f, 0.f, 0.f}, e = {0.f, 0.f, 0.f, 0.f};
#pragma unroll
            for (int ks = 0; ks < 4; ++ks) {
                d = mfma(*(const LAS v4u*)(L + qa0 + 64 * ks), fr[ks], d);
                e = mfma(*(const LAS v4u*)(L + qa1 + 64 * ks), fr[4 + ks], e);
            }
            unsigned sp = cvtpk(d[0], e[1]);
            const f32x2 s0 = unpk(sp) * 0.08838834764831845f;
            const f32x2 sc = unpk(cvtpk(s0.x, s0.y));
            float mx;
            asm("v_max_f32 %0, %1, %2" : "=v"(mx) : "v"(sc.x), "v"(sc.y));
            const float m = row16_max(mx) * 1.4426950408889634f;
            const float p0 = __builtin_amdgcn_exp2f(__builtin_fmaf(sc.x, 1.4426950408889634f, -m));
            const float p1 = __builtin_amdgcn_exp2f(__builtin_fmaf(sc.y, 1.4426950408889634f, -m));
            const unsigned ph = cvtpk(p0, p1);
            const unsigned pl = cvtpk(p0 - bflo(ph), p1 - bfhi(ph));
            LAS unsigned char* pw = L + L_PL + wv4 * 512 + qd * 128 + c16 * 2;
            *(LAS unsigned short*)pw = (unsigned short)ph;
            *(LAS unsigned short*)(pw + 32) = (unsigned short)(ph >> 16);
            *(LAS unsigned short*)(pw + 64) = (unsigned short)pl;
            *(LAS unsigned short*)(pw + 96) = (unsigned short)(pl >> 16);
            const float lsum = row16_sum(p0 + p1);
            const v4u pf = *(const LAS v4u*)(L + L_PL + wv4 * 512 + (c16 & 7) * 64 + 16 * qd);
#pragma unroll
            for (int t = 0; t < 8; ++t) {
                const f32x4 z = {0.f, 0.f, 0.f, 0.f};
                const f32x4 o = mfma(pf, fr[8 + t], z);
                if (lane < 32) *(LAS f32x2*)(L + L_OL + wv4 * 2048 + qd * 1024 + (16 * t + c16) * 8) = f32x2{o[0] + o[1], o[2] + o[3]};
            }
            if (c16 == 0) *(LAS f32x2*)(L + L_ML + (qd * 4 + wv4) * 8) = f32x2{m, lsum};
            STAMP(23 + layer);
        }
        __syncthreads();
        {
            const int h = wave >> 1, dim = tid & 127;
            const f32x2 ml = *(const LAS f32x2*)(L + L_ML + (h * 4 + (lane & 3)) * 8);
            float ov[4];
#pragma unroll
            for (int w = 0; w < 4; ++w) ov[w] = *(const LAS float*)(L + L_OL + w * 2048 + (h >> 1) * 1024 + dim * 8 + (h & 1) * 4);
            float M = ml.x;
            MAX_DPP(M, "row_ror:2"); MAX_DPP(M, "row_ror:1");
            const float f = __builtin_amdgcn_exp2f(ml.x - M);
            float o = 0.f;
#pragma unroll
            for (int w = 0; w < 4; ++w) o = __builtin_fmaf(__builtin_bit_cast(float, __builtin_amdgcn_readlane(__builtin_bit_cast(int, f), w)), ov[w], o);
            const unsigned slot = (unsigned)((g * 2 + (h >> 1)) * 32 + i);
            __builtin_amdgcn_raw_buffer_store_b32(canon(o), rs, WS_OP + layer * OP_L + slot * 1024 + (tid & 255) * 4, 0, AUX_SC1);
            float Ls = f * ml.y;
            Ls += dpp<ROR2>(Ls); Ls += dpp<ROR1>(Ls);
            if (dim == 0) __builtin_amdgcn_raw_buffer_store_b64(v2u{canon(M), canon(Ls)}, rs, WS_ML + layer * ML_L + slot * 16 + (h & 1) * 8, 0, AUX_SC1);
        }
        STAMP(5 + 8 * layer);
        if (layer == 0) {
            *(LAS v4u*)(L + L_WO + (32 * wave + (lane >> 2)) * 80 + 16 * (lane & 3)) = st0;
            *(LAS v4u*)(L + L_WO + (32 * wave + 16 + (lane >> 2)) * 80 + 16 * (lane & 3)) = st1;
            __syncthreads();
            if (wave < 2) {
#pragma unroll
                for (int ks = 0; ks < 8; ++ks) {
                    const LAS unsigned char* p = L + L_WO + (32 * ks + 8 * qd + (c16 >> 2)) * 80 + (16 * wave + 4 * (c16 & 3)) * 2;
                    const v2u lo = __builtin_bit_cast(v2u, __builtin_amdgcn_ds_read_tr16_b64_v4i16((LAS v4s*)p));
                    const v2u hi = __builtin_bit_cast(v2u, __builtin_amdgcn_ds_read_tr16_b64_v4i16((LAS v4s*)(p + 4 * 80)));
                    fr[4 + ks] = v4u{lo.x, lo.y, hi.x, hi.y};
                }
            }
            __builtin_amdgcn_s_sleep(D_E2A);
        } else {
            __builtin_amdgcn_s_sleep(D_E2);
        }
        {
            const int sp16 = lane >> 2, q4 = lane & 3;
            const unsigned ob = WS_OP + layer * OP_L + (team * 32 + sp16) * 1024 + (32 * wave + 4 * q4) * 4;
            const unsigned mb = WS_ML + layer * ML_L + (team * 32 + sp16) * 16 + (wave >> 2) * 8;
            v4u oa0, oa1, ob0, ob1; v2u mla, mlb;
#pragma unroll 1
            for (unsigned sp = 0; sp < SPIN_CAP; ++sp) {
                FENCE();
                oa0 = __builtin_amdgcn_raw_buffer_load_b128(rs, ob, 0, AUX_SC1);
                oa1 = __builtin_amdgcn_raw_buffer_load_b128(rs, ob + 64, 0, AUX_SC1);
                ob0 = __builtin_amdgcn_raw_buffer_load_b128(rs, ob + 16384, 0, AUX_SC1);
                ob1 = __builtin_amdgcn_raw_buffer_load_b128(rs, ob + 16384 + 64, 0, AUX_SC1);
                mla = __builtin_amdgcn_raw_buffer_load_b64(rs, mb, 0, AUX_SC1);
                mlb = __builtin_amdgcn_raw_buffer_load_b64(rs, mb + 256, 0, AUX_SC1);
                const unsigned mx = max(max(umax3(umax4(oa0), umax4(oa1), mla.x), umax3(umax4(ob0), umax4(ob1), mla.y)), max(mlb.x, mlb.y));
                if (__all(mx != SENT)) break;
                __builtin_amdgcn_s_sleep(2);
            }
            STAMP(6 + 8 * layer);
            const float ma = __uint_as_float(mla.x), mbv = __uint_as_float(mlb.x);
            float M;
            asm("v_max_f32 %0, %1, %2" : "=v"(M) : "v"(ma), "v"(mbv));
            MAX_DPP(M, "row_ror:8"); MAX_DPP(M, "row_ror:4");
            M = swap32_max(swap16_max(M));
            const float fa = __builtin_amdgcn_exp2f(ma - M), fb = __builtin_amdgcn_exp2f(mbv - M);
            float Ls = __builtin_fmaf(fa, __uint_as_float(mla.y), fb * __uint_as_float(mlb.y));
            Ls += dpp<ROR8>(Ls); Ls += dpp<ROR4>(Ls);
            Ls = swap16_sum(Ls, Ls); Ls = swap32_sum(Ls, Ls);
            const f32x4 a = __builtin_bit_cast(f32x4, oa0) * fa + __builtin_bit_cast(f32x4, ob0) * fb;
            const f32x4 c = __builtin_bit_cast(f32x4, oa1) * fa + __builtin_bit_cast(f32x4, ob1) * fb;
            float v[4];
#pragma unroll
            for (int k = 0; k < 4; ++k) v[k] = swap32_sum(a[k], c[k]);
            float w0 = swap16_sum(v[0], v[2]), w1v = swap16_sum(v[1], v[3]);
            w0 += dpp<ROR8>(w0); w1v += dpp<ROR8>(w1v);
            w0 += dpp<ROR4>(w0); w1v += dpp<ROR4>(w1v);
            const float rl = __builtin_amdgcn_rcpf(Ls);
            if ((lane & 12) == 0) *(LAS unsigned*)(L + L_OB + (32 * wave + 16 * (qd >> 1) + 4 * q4 + 2 * (qd & 1)) * 2) = cvtpk(w0 * rl, w1v * rl);
        }
        __syncthreads();
        if (wave < 2) {
            f32x4 d = {0.f, 0.f, 0.f, 0.f};
#pragma unroll
            for (int ks = 0; ks < 8; ++ks) d = mfma(*(const LAS v4u*)(L + L_OB + 64 * ks + 16 * qd), fr[4 + ks], d);
            if (lane < 16) __builtin_amdgcn_raw_buffer_store_b32(canon(d[0]), rs, WS_XP + layer * XP_L + (team * 512 + 32 * j + 16 * wave + lane) * 4, 0, AUX_SC1);
        }
        STAMP(7 + 8 * layer);
        if (layer == 1 && b != 0) break;
        if (layer == 0) {
            __builtin_amdgcn_s_sleep(D_E3A);
            v4u tv;
#pragma unroll 1
            for (unsigned sp = 0; sp < SPIN_CAP; ++sp) {
                FENCE();
                tv = __builtin_amdgcn_raw_buffer_load_b128(rs, WS_TAB + tid * 16, 0, AUX_SC1);
                const unsigned mx = max(umax4(tv), umax4(tv << 16));
                if (__all(mx < 0xFFFF0000u)) break;
                __builtin_amdgcn_s_sleep(2);
            }
            *(LAS v4u*)(L + L_TAB + tid * 16) = tv;
        } else {
            __builtin_amdgcn_s_sleep(D_E3);
        }
        {
            const unsigned xb = WS_XP + layer * XP_L + (4 * qd * 512 + 64 * wave + 4 * c16) * 4;
            v4u xv[4];
#pragma unroll 1
            for (unsigned sp = 0; sp < SPIN_CAP; ++sp) {
                FENCE();
#pragma unroll
                for (int k = 0; k < 4; ++k) xv[k] = __builtin_amdgcn_raw_buffer_load_b128(rs, xb + k * 2048, 0, AUX_SC1);
                const unsigned mx = max(max(umax4(xv[0]), umax4(xv[1])), max(umax4(xv[2]), umax4(xv[3])));
                if (__all(mx != SENT)) break;
                __builtin_amdgcn_s_sleep(2);
            }
            const f32x4 v = (__builtin_bit_cast(f32x4, xv[0]) + __builtin_bit_cast(f32x4, xv[1])) + (__builtin_bit_cast(f32x4, xv[2]) + __builtin_bit_cast(f32x4, xv[3]));
            const float a0 = swap32_sum(v[0], v[2]), a1 = swap32_sum(v[1], v[3]);
            const float a = swap16_sum(a0, a1);
            *(LAS float*)(L + L_AL + (64 * wave + 4 * c16 + qd) * 4) = rbf(a);
        }
        __syncthreads();
        STAMP(8 + 8 * layer);
        if (wave < 4) {
            const f32x4 a0 = *(const LAS f32x4*)(L + L_AL + 32 * lg), a1 = *(const LAS f32x4*)(L + L_AL + 32 * lg + 16);
            { f32x2 t = unpk(xp.x) + f32x2{a0[0], a0[1]}; xp.x = cvtpk(t.x, t.y); }
            { f32x2 t = unpk(xp.y) + f32x2{a0[2], a0[3]}; xp.y = cvtpk(t.x, t.y); }
            { f32x2 t = unpk(xp.z) + f32x2{a1[0], a1[1]}; xp.z = cvtpk(t.x, t.y); }
            { f32x2 t = unpk(xp.w) + f32x2{a1[2], a1[3]}; xp.w = cvtpk(t.x, t.y); }
            const v4u h2 = layernorm_pk(layernorm_pk(xp));
            unsigned yv[8], big = 0;
#pragma unroll
            for (int k = 0; k < 8; ++k) {
                const unsigned u = (k & 1) ? (h2[k >> 1] >> 16) : (h2[k >> 1] & 0xFFFFu);
                const unsigned rel = (u & 0x7FFFu) - (unsigned)(TAB_E0 << 7);
                big = max(big, rel);
                const unsigned idx = ((u >> 15) << 11) | (rel & 2047u);
                yv[k] = *(const LAS unsigned short*)(L + L_TAB + idx * 2);
            }
            if (__any(big >= 2048u)) {
#pragma unroll 1
                for (int k = 0; k < 8; ++k) {
                    unsigned hw = h2[0];
#pragma unroll
                    for (int kk = 1; kk < 4; ++kk) hw = ((k >> 1) == kk) ? h2[kk] : hw;
                    const unsigned u = (k & 1) ? (hw >> 16) : (hw & 0xFFFFu);
                    unsigned long long mk = __ballot(((u & 0x7FFFu) - (unsigned)(TAB_E0 << 7)) >= 2048u);
#pragma unroll 1
                    while (mk) {
                        const int ln = __builtin_ctzll(mk);
                        mk &= mk - 1;
                        const float tv = __uint_as_float((unsigned)__builtin_amdgcn_readlane((int)u, ln) << 16);
                        float a = 0.f;
#pragma unroll 1
                        for (int p = lane; p < FF / 2; p += 64) {
                            const unsigned ww1 = *(const LAS unsigned*)(L + L_MW + 4 * p), bb1 = *(const LAS unsigned*)(L + L_MW + 3584 + 4 * p), ww2 = *(const LAS unsigned*)(L + L_MW + 7168 + 4 * p);
                            a = mlp_pair(tv, unpk(ww1), unpk(bb1), ww2, a);
                        }
                        const unsigned y = cvtpk(rbf(wave_sum(a)) + b2f, 0.f) & 0xFFFFu;
#pragma unroll
                        for (int kk = 0; kk < 8; ++kk) yv[kk] = (k == kk && lane == ln) ? y : yv[kk];
                    }
                }
            }
#pragma unroll
            for (int k = 0; k < 4; ++k) {
                const f32x2 t = unpk(xp[k]) + f32x2{__uint_as_float(yv[2 * k] << 16), __uint_as_float(yv[2 * k + 1] << 16)};
                xp[k] = cvtpk(t.x, t.y);
            }
            if (layer == 0) {
                stage_q(layernorm_pk(xp), 1);
                STAMP(9);
            } else if (wave == 0) {
                *(v4u*)(out + 8 * lg) = xp;
            }
        }
    }
    STAMP(17);
    STAMP_DUMP();
}

extern "C" void kernel_launch(void* const* d_in, const int* in_sizes, int n_in, void* d_out, int out_size, void* d_ws,
                              size_t ws_size, hipStream_t stream) {
    const bf16_t* x = (const bf16_t*)d_in[0];
    const bf16_t* kc = (const bf16_t*)d_in[1];
    const bf16_t* vc = (const bf16_t*)d_in[2];
    const bf16_t* wq = (const bf16_t*)d_in[3];
    const bf16_t* wo = (const bf16_t*)d_in[4];
    const bf16_t* w1 = (const bf16_t*)d_in[6];
    const bf16_t* b1 = (const bf16_t*)d_in[7];
    const bf16_t* w2 = (const bf16_t*)d_in[8];
    const bf16_t* b2 = (const bf16_t*)d_in[9];
    (void)hipFuncSetAttribute((const void*)fused_kernel, hipFuncAttributeMaxDynamicSharedMemorySize, LDS_BYTES);
    fused_kernel<<<NWG, NTHR, LDS_BYTES, stream>>>(x, kc, vc, wq, wo, w1, b1, w2, b2, (bf16_t*)d_out, d_ws);
}
```

```cpp
#include <hip/hip_runtime.h>
#include <hip/hip_bf16.h>
#include <stdint.h>

typedef unsigned short bf16_t;
typedef unsigned v4u __attribute__((ext_vector_type(4)));
typedef unsigned v2u __attribute__((ext_vector_type(2)));
typedef float f32x4 __attribute__((ext_vector_type(4)));
typedef float f32x2 __attribute__((ext_vector_type(2)));
typedef short bf16x8 __attribute__((ext_vector_type(8)));
typedef short v4s __attribute__((ext_vector_type(4)));
typedef __bf16 bf16x2 __attribute__((ext_vector_type(2)));
#define LAS __attribute__((address_space(3)))

#define D_E1 10
#define D_E2 12
#define D_E3 18
#define D_K0 12
#define D_FL 70
#define D_Q0 8
#define TABK 7
#define KB1 31
#define AUX_MW 0
#define INFL 32
#define S1_STEP 2
#define WO_STEP 3
#define D_V0 0
#define D_FR 8
#define D_E2A 20
#define D_E2X 6
#define D_E3A 8

constexpr int DM = 512, SK = 65536, WIN = 4096, FF = 1792;
constexpr int NWG = 256, NTHR = 640;
constexpr unsigned SENT = 0xFFFFFFFFu;
constexpr int AUX_SC1 = 16, AUX_NT = 2;
constexpr unsigned SPIN_CAP = 1u << 20;

constexpr unsigned WS_EPOCH = 0;
constexpr unsigned WS_FLAGS = 4096;
constexpr unsigned WS_TAB = 8192;
constexpr unsigned WS_QP = 16384;
constexpr unsigned QP_L = 8 * 4 * 512 * 4;
constexpr unsigned WS_OP = WS_QP + 2 * QP_L;
constexpr unsigned OP_L = 8 * 2 * 32 * 256 * 4;
constexpr unsigned WS_ML = WS_OP + 2 * OP_L;
constexpr unsigned ML_L = 8 * 2 * 32 * 4 * 4;
constexpr unsigned WS_XP = WS_ML + 2 * ML_L;
constexpr unsigned XP_L = 16 * 512 * 4;
constexpr unsigned WS_END = WS_XP + 2 * XP_L;
constexpr unsigned WS_STAMP = 4u << 20;

constexpr int L_K = 0;
constexpr int L_V = 32768;
constexpr int L_WQ = 65536;
constexpr int L_WO = L_WQ + 16384;
constexpr int L_OL = L_WO + 256 * 80;
constexpr int L_PL = L_OL + 8192;
constexpr int L_AL = L_PL + 2048;
constexpr int L_OB = L_AL + 2048;
constexpr int L_ML = L_OB + 512;
constexpr int L_QS = L_ML + 128;
constexpr int L_TL = L_QS + 1280;
constexpr int L_END = L_TL + 128 + 16;
constexpr int LDS_BYTES = 144 * 1024;
static_assert(L_END + 2560 <= LDS_BYTES, "lds");
static_assert(L_QS % 16 == 0 && L_TL % 16 == 0 && L_OL % 16 == 0, "align");
constexpr int L_TABP = 2 * (114 << 7), L_TABN = 2 * (0x8000 + (114 << 7));
static_assert(L_TABP + 4096 <= L_WQ && L_TABN >= L_WO && L_TABN + 4096 <= L_OL && 131072 <= LDS_BYTES, "table placement");

constexpr int TAB_E0 = 114;

#define FENCE() asm volatile("" ::: "memory")

__device__ __forceinline__ float bflo(unsigned p) { return __uint_as_float(p << 16); }
__device__ __forceinline__ float bfhi(unsigned p) { return __uint_as_float(p & 0xFFFF0000u); }
__device__ __forceinline__ unsigned cvtpk(float a, float b) {
    f32x2 v = {a, b};
    return __builtin_bit_cast(unsigned, __builtin_convertvector(v, bf16x2));
}
__device__ __forceinline__ float rbf(float a) { return bflo(cvtpk(a, a)); }
__device__ __forceinline__ float dot2(unsigned a, unsigned b, float c) {
    return __builtin_amdgcn_fdot2_f32_bf16(__builtin_bit_cast(bf16x2, a), __builtin_bit_cast(bf16x2, b), c, false);
}
template <int CTRL> __device__ __forceinline__ float dpp(float x) {
    return __builtin_bit_cast(float, __builtin_amdgcn_mov_dpp(__builtin_bit_cast(int, x), CTRL, 0xf, 0xf, true));
}
constexpr int ROR1 = 0x121, ROR2 = 0x122, ROR4 = 0x124, ROR8 = 0x128;
__device__ __forceinline__ float swap32_sum(float a, float b) {
    auto r = __builtin_amdgcn_permlane32_swap(__float_as_uint(a), __float_as_uint(b), false, false);
    return __uint_as_float(r[0]) + __uint_as_float(r[1]);
}
__device__ __forceinline__ float swap16_sum(float a, float b) {
    auto r = __builtin_amdgcn_permlane16_swap(__float_as_uint(a), __float_as_uint(b), false, false);
    return __uint_as_float(r[0]) + __uint_as_float(r[1]);
}
__device__ __forceinline__ float swap32_max(float a) {
    auto r = __builtin_amdgcn_permlane32_swap(__float_as_uint(a), __float_as_uint(a), false, false);
    float o;
    asm("v_max_f32 %0, %1, %2" : "=v"(o) : "v"(r[0]), "v"(r[1]));
    return o;
}
__device__ __forceinline__ float swap16_max(float a) {
    auto r = __builtin_amdgcn_permlane16_swap(__float_as_uint(a), __float_as_uint(a), false, false);
    float o;
    asm("v_max_f32 %0, %1, %2" : "=v"(o) : "v"(r[0]), "v"(r[1]));
    return o;
}
__device__ __forceinline__ float row16_sum(float v) {
    v += dpp<ROR8>(v); v += dpp<ROR4>(v); v += dpp<ROR2>(v); v += dpp<ROR1>(v);
    return v;
}
#define MAX_DPP(v, CTRL) asm volatile("s_nop 1\n\tv_max_f32_dpp %0, %0, %0 " CTRL " row_mask:0xf bank_mask:0xf" : "+v"(v))
__device__ __forceinline__ float row16_max(float v) {
    MAX_DPP(v, "row_ror:8"); MAX_DPP(v, "row_ror:4"); MAX_DPP(v, "row_ror:2"); MAX_DPP(v, "row_ror:1");
    return v;
}
__device__ __forceinline__ float row8_max(float v) {
    MAX_DPP(v, "row_ror:4"); MAX_DPP(v, "row_ror:2"); MAX_DPP(v, "row_ror:1");
    return v;
}
__device__ __forceinline__ float wave_sum(float v) { v = row16_sum(v); v = swap16_sum(v, v); return swap32_sum(v, v); }
__device__ __forceinline__ unsigned canon(float v) {
    unsigned u = __float_as_uint(v);
    return u < SENT ? u : 0x7FC00000u;
}
__device__ __forceinline__ unsigned umax3(unsigned a, unsigned b, unsigned c) { return max(max(a, b), c); }
__device__ __forceinline__ unsigned umax4(v4u a) { return max(umax3(a.x, a.y, a.z), a.w); }
__device__ __forceinline__ f32x4 mfma(v4u a, v4u b, f32x4 c) {
    return __builtin_amdgcn_mfma_f32_16x16x32_bf16(__builtin_bit_cast(bf16x8, a), __builtin_bit_cast(bf16x8, b), c, 0, 0, 0);
}
__device__ __forceinline__ float mlp_pair(float t, f32x2 w1, f32x2 b1, unsigned w2p, float acc) {
    const f32x2 c1 = {-2.3022081986f, -2.3022081986f}, c2 = {-0.1029432396f, -0.1029432396f};
    const f32x2 tt = {t, t};
    const f32x2 a = tt * w1;
    const unsigned p = cvtpk(a.x, a.y);
    f32x2 u = {bflo(p), bfhi(p)};
    u += b1;
    const unsigned q = cvtpk(u.x, u.y);
    u.x = bflo(q); u.y = bfhi(q);
    const f32x2 z = u * __builtin_elementwise_fma(u * u, c2, c1);
    f32x2 e = {__builtin_amdgcn_exp2f(z.x), __builtin_amdgcn_exp2f(z.y)};
    e += 1.0f;
    const f32x2 r = {__builtin_amdgcn_rcpf(e.x), __builtin_amdgcn_rcpf(e.y)};
    const f32x2 gl = u * r;
    return dot2(cvtpk(gl.x, gl.y), w2p, acc);
}

__device__ __forceinline__ void mlp_pair2(float t0, float t1, f32x2 w1, f32x2 b1, unsigned w2p, float& acc0, float& acc1) {
    const f32x2 c1 = {-2.3022081986f, -2.3022081986f}, c2 = {-0.1029432396f, -0.1029432396f};
    const f32x2 a0 = f32x2{t0, t0} * w1, a1 = f32x2{t1, t1} * w1;
    const unsigned p0 = cvtpk(a0.x, a0.y), p1 = cvtpk(a1.x, a1.y);
    f32x2 u0 = {bflo(p0), bfhi(p0)}, u1 = {bflo(p1), bfhi(p1)};
    u0 += b1; u1 += b1;
    const unsigned q0 = cvtpk(u0.x, u0.y), q1 = cvtpk(u1.x, u1.y);
    u0 = f32x2{bflo(q0), bfhi(q0)}; u1 = f32x2{bflo(q1), bfhi(q1)};
    const f32x2 s0 = u0 * u0, s1 = u1 * u1;
    const f32x2 y0 = __builtin_elementwise_fma(s0, c2, c1), y1 = __builtin_elementwise_fma(s1, c2, c1);
    const f32x2 z0 = u0 * y0, z1 = u1 * y1;
    f32x2 e0 = {__builtin_amdgcn_exp2f(z0.x), __builtin_amdgcn_exp2f(z0.y)}, e1 = {__builtin_amdgcn_exp2f(z1.x), __builtin_amdgcn_exp2f(z1.y)};
    e0 += 1.0f; e1 += 1.0f;
    const f32x2 r0 = {__builtin_amdgcn_rcpf(e0.x), __builtin_amdgcn_rcpf(e0.y)}, r1 = {__builtin_amdgcn_rcpf(e1.x), __builtin_amdgcn_rcpf(e1.y)};
    const f32x2 g0 = u0 * r0, g1 = u1 * r1;
    const unsigned h0 = cvtpk(g0.x, g0.y), h1 = cvtpk(g1.x, g1.y);
    acc0 = dot2(h0, w2p, acc0); acc1 = dot2(h1, w2p, acc1);
}
__device__ __forceinline__ f32x2 unpk(unsigned p) { return f32x2{bflo(p), bfhi(p)}; }
__device__ __forceinline__ v4u layernorm_pk(v4u xp) {
    const unsigned ONES = 0x3F803F80u;
    float s1 = dot2(xp.x, ONES, 0.f), s2 = dot2(xp.x, xp.x, 0.f);
    s1 = dot2(xp.y, ONES, s1); s2 = dot2(xp.y, xp.y, s2);
    s1 = dot2(xp.z, ONES, s1); s2 = dot2(xp.z, xp.z, s2);
    s1 = dot2(xp.w, ONES, s1); s2 = dot2(xp.w, xp.w, s2);
    f32x2 d[4];
#pragma unroll
    for (int k = 0; k < 4; ++k) d[k] = unpk(xp[k]);
    s1 += dpp<ROR8>(s1); s2 += dpp<ROR8>(s2);
    s1 += dpp<ROR4>(s1); s2 += dpp<ROR4>(s2);
    s1 += dpp<ROR2>(s1); s2 += dpp<ROR2>(s2);
    s1 += dpp<ROR1>(s1); s2 += dpp<ROR1>(s2);
    float t = swap16_sum(s1, s2);
    t = swap32_sum(t, t);
    auto r2 = __builtin_amdgcn_permlane16_swap(__float_as_uint(t), __float_as_uint(t), false, false);
    const float mean = __uint_as_float(r2[0]) * (1.0f / DM);
    const float msq = mean * mean;
    float var = __builtin_fmaf(__uint_as_float(r2[1]), 1.0f / DM, -msq);
    const float m = rbf(mean);
    const f32x2 mm = {m, m};
#pragma unroll
    for (int k = 0; k < 4; ++k) d[k] -= mm;
    if (__builtin_expect(!(msq <= 64.f * var), 0)) {
        f32x2 q = d[0] * d[0];
#pragma unroll
        for (int k = 1; k < 4; ++k) q = __builtin_elementwise_fma(d[k], d[k], q);
        const float dm = mean - m;
        var = fmaxf(__builtin_fmaf(-dm, dm, wave_sum(q.x + q.y) * (1.0f / DM)), 0.f);
    }
    const float sd = rbf(__builtin_amdgcn_sqrtf(rbf(rbf(var) + 1e-5f)));
    const float r = __builtin_amdgcn_rcpf(sd);
    const f32x2 rr = {r, r};
    v4u o;
#pragma unroll
    for (int k = 0; k < 4; ++k) {
        const f32x2 tt = unpk(cvtpk(d[k].x, d[k].y)) * rr;
        o[k] = cvtpk(tt.x, tt.y);
    }
    return o;
}

#define STAMP(n) do { } while (0)
#define STAMP_DUMP() do { } while (0)
#define WAITVM(n) do { } while (0)

__global__ void __launch_bounds__(NTHR, 3)
fused_kernel(const bf16_t* x, const bf16_t* kc, const bf16_t* vc, const bf16_t* wq, const bf16_t* wo, const bf16_t* w1,
             const bf16_t* b1, const bf16_t* w2, const bf16_t* b2, bf16_t* out, void* ws) {
    extern __shared__ __attribute__((aligned(16))) unsigned char smem[];
    LAS unsigned char* L = (LAS unsigned char*)smem;
    const int b = blockIdx.x, tid = threadIdx.x, lane = tid & 63;
    const int wave = __builtin_amdgcn_readfirstlane(tid >> 6);
    const int g = b & 7, i = b >> 3, hp = i & 1, j = i >> 1, cb = i & 7, rb = i >> 3;
    const __amdgpu_buffer_rsrc_t rs = __builtin_amdgcn_make_buffer_rsrc(ws, 0, (int)WS_END, 0x00020000);
    const unsigned team = (unsigned)(g * 2 + hp);
    STAMP(0);

    auto gather_q = [&](int layer) {
        const unsigned base = WS_QP + layer * QP_L + g * 8192 + lane * 16;
        v4u qv[8];
#pragma unroll 1
        for (unsigned sp = 0; sp < SPIN_CAP; ++sp) {
            FENCE();
#pragma unroll
            for (int k = 0; k < 8; ++k) qv[k] = __builtin_amdgcn_raw_buffer_load_b128(rs, base + k * 1024, 0, AUX_SC1);
            unsigned mx = 0;
#pragma unroll
            for (int k = 0; k < 8; ++k) mx = max(mx, umax4(qv[k]));
            if (__all(mx != SENT)) break;
            __builtin_amdgcn_s_sleep(2);
        }
#pragma unroll
        for (int hf = 0; hf < 2; ++hf) {
            const f32x4 q = (__builtin_bit_cast(f32x4, qv[hf]) + __builtin_bit_cast(f32x4, qv[2 + hf])) + (__builtin_bit_cast(f32x4, qv[4 + hf]) + __builtin_bit_cast(f32x4, qv[6 + hf]));
            *(LAS v2u*)(L + L_QS + hf * 512 + lane * 8) = v2u{cvtpk(q[0], q[1]), cvtpk(q[2], q[3])};
        }
    };

    if (wave == 9) {
        __builtin_amdgcn_s_setprio(3);
        const size_t kvb = ((size_t)g * SK + (SK - WIN) + 128 * i) * 128;
        const __amdgpu_buffer_rsrc_t rk = __builtin_amdgcn_make_buffer_rsrc((void*)(kc + kvb), 0, 32768, 0x00020000);
        const __amdgpu_buffer_rsrc_t rv = __builtin_amdgcn_make_buffer_rsrc((void*)(vc + kvb), 0, 32768, 0x00020000);
        int ko[4], vo[4];
#pragma unroll
        for (int k = 0; k < 4; ++k) {
            const int row = 4 * k + (lane >> 4);
            ko[k] = row * 256 + 16 * ((lane & 15) ^ row);
            vo[k] = row * 256 + 16 * ((lane & 15) ^ (2 * ((row & 3) | ((row >> 3) << 2))));
        }
        __builtin_amdgcn_s_barrier();
        __builtin_amdgcn_s_sleep(D_K0);
#pragma unroll
        for (int it = 0; it < 64; ++it) {
            if (it < 32) __builtin_amdgcn_raw_ptr_buffer_load_lds(rk, (LAS unsigned*)(L + L_K + it * 1024), 16, ko[it & 3], (it >> 2) * 4096, 0, AUX_NT);
            else __builtin_amdgcn_raw_ptr_buffer_load_lds(rv, (LAS unsigned*)(L + L_V + (it - 32) * 1024), 16, vo[it & 3], ((it - 32) >> 2) * 4096, 0, AUX_NT);
            if (it == KB1) { __builtin_amdgcn_s_barrier(); __builtin_amdgcn_s_sleep(D_V0); }
        }
        asm volatile("s_waitcnt vmcnt(0)" ::: "memory");
        STAMP(20);
        __builtin_amdgcn_s_barrier();
        STAMP_DUMP();
        return;
    }
    if (wave == 8) {
        __builtin_amdgcn_s_setprio(3);
        const v2u ep = __builtin_amdgcn_raw_buffer_load_b64(rs, WS_EPOCH, 0, AUX_SC1);
        const v4u sv = {SENT, SENT, SENT, SENT};
#pragma unroll
        for (int l2 = 0; l2 < 2; ++l2) {
            __builtin_amdgcn_raw_buffer_store_b128(sv, rs, WS_OP + l2 * OP_L + ((g * 2) * 32 + i) * 1024 + lane * 16, 0, AUX_SC1);
            __builtin_amdgcn_raw_buffer_store_b128(sv, rs, WS_OP + l2 * OP_L + ((g * 2 + 1) * 32 + i) * 1024 + lane * 16, 0, AUX_SC1);
            unsigned off;
            if (lane < 16) off = WS_QP + l2 * QP_L + ((g * 4 + rb) * 512 + cb * 64) * 4 + lane * 16;
            else if (lane < 24) off = WS_XP + l2 * XP_L + (team * 512 + j * 32) * 4 + (lane - 16) * 16;
            else if (lane < 26) off = WS_ML + l2 * ML_L + ((g * 2 + (lane - 24)) * 32 + i) * 16;
            else off = WS_TAB + b * 32 + (lane & 1) * 16;
            if (lane < 28) __builtin_amdgcn_raw_buffer_store_b128(sv, rs, off, 0, AUX_SC1);
        }
        *(LAS unsigned*)(L + L_QS + 1024 + lane * 4) = 0u;
        FENCE();
        __builtin_amdgcn_s_barrier();
        FENCE();
        const unsigned long long T = (((unsigned long long)ep.y << 32) | ep.x) + 0x9E3779B97F4A7C15ull;
        const unsigned tlo = __builtin_amdgcn_readfirstlane((unsigned)T), thi = __builtin_amdgcn_readfirstlane((unsigned)(T >> 32));
        asm volatile("s_waitcnt vmcnt(0)" ::: "memory");
        STAMP(20);
        if (lane == 0) __builtin_amdgcn_raw_buffer_store_b64(v2u{tlo, thi}, rs, WS_FLAGS + b * 8, 0, AUX_SC1);
        __syncthreads();
        __builtin_amdgcn_s_sleep(D_FL);
        const unsigned qbase = WS_QP + g * 8192 + lane * 16;
        v4u qv[8];
#pragma unroll 1
        for (unsigned sp = 0; sp < SPIN_CAP; ++sp) {
            FENCE();
            const v4u f0 = __builtin_amdgcn_raw_buffer_load_b128(rs, WS_FLAGS + lane * 32, 0, AUX_SC1);
            const v4u f1 = __builtin_amdgcn_raw_buffer_load_b128(rs, WS_FLAGS + lane * 32 + 16, 0, AUX_SC1);
#pragma unroll
            for (int k = 0; k < 8; ++k) qv[k] = __builtin_amdgcn_raw_buffer_load_b128(rs, qbase + k * 1024, 0, AUX_SC1);
            const unsigned bad = (f0.x ^ tlo) | (f0.y ^ thi) | (f0.z ^ tlo) | (f0.w ^ thi) | (f1.x ^ tlo) | (f1.y ^ thi) | (f1.z ^ tlo) | (f1.w ^ thi);
            unsigned mx = 0;
#pragma unroll
            for (int k = 0; k < 8; ++k) mx = max(mx, umax4(qv[k]));
            if (__all(bad == 0u && mx != SENT)) break;
            __builtin_amdgcn_s_sleep(D_FR);
        }
        if (b == 0 && lane == 0) __builtin_amdgcn_raw_buffer_store_b64(v2u{tlo, thi}, rs, WS_EPOCH, 0, AUX_SC1);
        STAMP(21);
#pragma unroll 1
        for (unsigned sp = 0; sp < SPIN_CAP; ++sp) {
            FENCE();
            v4u rv2[8];
#pragma unroll
            for (int k = 0; k < 8; ++k) rv2[k] = __builtin_amdgcn_raw_buffer_load_b128(rs, qbase + k * 1024, 0, AUX_SC1);
            FENCE();
#pragma unroll
            for (int hf = 0; hf < 2; ++hf) {
                const f32x4 q = (__builtin_bit_cast(f32x4, qv[hf]) + __builtin_bit_cast(f32x4, qv[2 + hf])) + (__builtin_bit_cast(f32x4, qv[4 + hf]) + __builtin_bit_cast(f32x4, qv[6 + hf]));
                *(LAS v2u*)(L + L_QS + hf * 512 + lane * 8) = v2u{cvtpk(q[0], q[1]), cvtpk(q[2], q[3])};
            }
            asm volatile("s_waitcnt lgkmcnt(0)" ::: "memory");
            __builtin_amdgcn_s_barrier();
            FENCE();
            unsigned diff = 0;
#pragma unroll
            for (int k = 0; k < 8; ++k) { const v4u d = rv2[k] ^ qv[k]; diff |= d.x | d.y | d.z | d.w; qv[k] = rv2[k]; }
            const bool same = __all(diff == 0u);
            if (!same) {
#pragma unroll 1
                for (unsigned s2 = 0; s2 < SPIN_CAP; ++s2) {
                    unsigned mx = 0;
#pragma unroll
                    for (int k = 0; k < 8; ++k) mx = max(mx, umax4(qv[k]));
                    if (__all(mx != SENT)) break;
                    __builtin_amdgcn_s_sleep(4);
                    FENCE();
#pragma unroll
                    for (int k = 0; k < 8; ++k) qv[k] = __builtin_amdgcn_raw_buffer_load_b128(rs, qbase + k * 1024, 0, AUX_SC1);
                }
            }
            if (lane == 0) *(LAS unsigned*)(L + L_TL + 128) = same ? 1u : 0u;
            __syncthreads();
            if (same) break;
        }
        STAMP(22);
        STAMP_DUMP();
        return;
    }

    const int qd = lane >> 4, c16 = lane & 15, wv4 = wave & 3;
    const int lg = 16 * (c16 >> 2) + 4 * (c16 & 3) + qd;
    const int pg = tid & 127, eg = tid >> 7;
    unsigned w1p[7], b1p[7], w2p[7];
#pragma unroll
    for (int k = 0; k < 7; ++k) {
        w1p[k] = *(const unsigned*)(w1 + 2 * (pg + 128 * k));
        b1p[k] = *(const unsigned*)(b1 + 2 * (pg + 128 * k));
        w2p[k] = *(const unsigned*)(w2 + 2 * (pg + 128 * k));
    }
    const float b2f = __uint_as_float((unsigned)b2[0] << 16);
    const v4u xin = *(const v4u*)(x + 8 * lg);
    FENCE();
    const __amdgpu_buffer_rsrc_t rwq = __builtin_amdgcn_make_buffer_rsrc((void*)(wq + (size_t)(128 * rb) * 4096 + 512 * g + 64 * cb), 0, 128 * 8192, 0x00020000);
    v4u st0, st1;
    st0 = __builtin_amdgcn_raw_buffer_load_b128(rwq, (16 * wave + (lane >> 3)) * 8192 + 16 * (lane & 7), 0, AUX_NT);
    st1 = __builtin_amdgcn_raw_buffer_load_b128(rwq, (16 * wave + (lane >> 3) + 8) * 8192 + 16 * (lane & 7), 0, AUX_NT);
    FENCE();
    __builtin_amdgcn_s_barrier();
    FENCE();
    v4u xp = xin;
    v4u fr[16];
    const unsigned qp_off = WS_QP + ((g * 4 + rb) * 512 + cb * 64 + 16 * wv4 + c16) * 4;
    auto stage_q = [&](v4u h, int layer) {
        const f32x4 z = {0.f, 0.f, 0.f, 0.f};
        const f32x4 d0 = mfma(h, fr[0], z), d1 = mfma(h, fr[1], z), d2 = mfma(h, fr[2], z), d3 = mfma(h, fr[3], z);
        const float q = (d0[0] + d1[1]) + (d2[2] + d3[3]);
        if (qd == rb) __builtin_amdgcn_raw_buffer_store_b32(canon(q), rs, qp_off + layer * QP_L, 0, AUX_SC1);
    };

    v4u hn0 = {0u, 0u, 0u, 0u};
    if (wave < 4) hn0 = layernorm_pk(xp);
    {
        f32x2 w1f[7], b1f[7];
#pragma unroll
        for (int k = 0; k < 7; ++k) { w1f[k] = unpk(w1p[k]); b1f[k] = unpk(b1p[k]); }
        float acc[4] = {0.f, 0.f, 0.f, 0.f};
#pragma unroll
        for (int u = 0; u < 4; u += 2) {
            const unsigned idx = (unsigned)(16 * b + 4 * eg + u);
            const float tv0 = __uint_as_float((((idx >> 11) << 15) | ((idx & 0x7FFu) + (TAB_E0 << 7))) << 16);
            const float tv1 = __uint_as_float(__float_as_uint(tv0) + 0x10000u);
#pragma unroll
            for (int k = 0; k < TABK; ++k) {
                if (u == 0 && k == S1_STEP) {
                    asm volatile("" : "+v"(acc[0]), "+v"(acc[1]) :: "memory");
                    *(LAS v4u*)(L + L_WQ + (16 * wave + (lane >> 3)) * 128 + 16 * (lane & 7)) = st0;
                    *(LAS v4u*)(L + L_WQ + (16 * wave + (lane >> 3) + 8) * 128 + 16 * (lane & 7)) = st1;
                    STAMP(20);
                    __syncthreads();
                    if (wave < 4) {
                        __builtin_amdgcn_s_setprio(2);
#pragma unroll
                        for (int ks = 0; ks < 4; ++ks) {
                            const LAS unsigned char* p = L + L_WQ + (32 * ks + 8 * qd + (c16 >> 2)) * 128 + (16 * wv4 + 4 * (c16 & 3)) * 2;
                            const v2u lo = __builtin_bit_cast(v2u, __builtin_amdgcn_ds_read_tr16_b64_v4i16((LAS v4s*)p));
                            const v2u hi = __builtin_bit_cast(v2u, __builtin_amdgcn_ds_read_tr16_b64_v4i16((LAS v4s*)(p + 4 * 128)));
                            fr[ks] = v4u{lo.x, lo.y, hi.x, hi.y};
                        }
                        stage_q(hn0, 0);
                        __builtin_amdgcn_s_setprio(0);
                    }
                    STAMP(2);
                    FENCE();
                    __builtin_amdgcn_sched_barrier(0);
                }
                mlp_pair2(tv0, tv1, w1f[k], b1f[k], w2p[k], acc[u], acc[u + 1]);
                __builtin_amdgcn_sched_barrier(0);
            }
        }
        acc[0] = swap32_sum(acc[0], acc[2]); acc[1] = swap32_sum(acc[1], acc[3]);
        acc[0] = row16_sum(swap16_sum(acc[0], acc[1]));
        if (c16 == 0) *(LAS float*)(L + L_TL + (wave * 4 + qd) * 4) = acc[0];
    }
    STAMP(25);
    __syncthreads();
    STAMP(3);
    {
        const __amdgpu_buffer_rsrc_t rwo = __builtin_amdgcn_make_buffer_rsrc((void*)(wo + (size_t)(512 * g + 256 * hp) * 512 + 32 * j), 0, 256 * 1024, 0x00020000);
        st0 = __builtin_amdgcn_raw_buffer_load_b128(rwo, (32 * wave + (lane >> 2)) * 1024 + 16 * (lane & 3), 0, AUX_NT);
        st1 = __builtin_amdgcn_raw_buffer_load_b128(rwo, (32 * wave + 16 + (lane >> 2)) * 1024 + 16 * (lane & 3), 0, AUX_NT);
        FENCE();
    }
    if (wave >= 4) {
#pragma unroll
        for (int T = 0; T < 2; ++T)
#pragma unroll
            for (int ks = 0; ks < 4; ++ks)
                fr[4 * T + ks] = *(const LAS v4u*)(L + L_K + (32 * wv4 + 16 * T + c16) * 256 + 16 * ((4 * ks + qd) ^ c16));
    } else if (tid < 16) {
        const int e2 = tid >> 2, u = tid & 3;
        const float sum = *(const LAS float*)(L + L_TL + ((2 * e2) * 4 + u) * 4) + *(const LAS float*)(L + L_TL + ((2 * e2 + 1) * 4 + u) * 4);
        unsigned y = cvtpk(rbf(sum) + b2f, 0.f) & 0xFFFFu;
        if (y == 0xFFFFu) y = 0x7FC0u;
        __builtin_amdgcn_raw_buffer_store_b16((unsigned short)y, rs, WS_TAB + (16 * b + tid) * 2, 0, AUX_SC1);
    }
    const int qa0 = L_QS + ((c16 & 3) == 0 ? (c16 >> 2) * 256 : 1024) + 16 * qd;
    const int qa1 = L_QS + ((c16 & 3) == 1 ? (c16 >> 2) * 256 : 1024) + 16 * qd;

#pragma unroll
    for (int layer = 0; layer < 2; ++layer) {
        if (layer == 1) {
            if (wave == 0) { __builtin_amdgcn_s_sleep(D_E1); gather_q(1); }
            __syncthreads();
        }
        STAMP(4 + 8 * layer);
#pragma unroll 1
        for (unsigned redo = 0; redo < SPIN_CAP; ++redo) {
        if (wave >= 4) {
            v4u qf[8];
#pragma unroll
            for (int ks = 0; ks < 4; ++ks) { qf[2 * ks] = *(const LAS v4u*)(L + qa0 + 64 * ks); qf[2 * ks + 1] = *(const LAS v4u*)(L + qa1 + 64 * ks); }
            if (layer == 0 && redo == 0) {
                const int vrow = 32 * wv4 + 8 * qd + (c16 >> 2);
                const int vsw = 2 * ((vrow & 3) | (((vrow >> 3) & 1) << 2));
                const LAS unsigned char* vb = L + L_V + vrow * 256 + 8 * (c16 & 1);
#pragma unroll
                for (int t = 0; t < 8; ++t) {
                    const LAS unsigned char* p = vb + 16 * ((2 * t + ((c16 >> 1) & 1)) ^ vsw);
                    const v2u lo = __builtin_bit_cast(v2u, __builtin_amdgcn_ds_read_tr16_b64_v4i16((LAS v4s*)p));
                    const v2u hi = __builtin_bit_cast(v2u, __builtin_amdgcn_ds_read_tr16_b64_v4i16((LAS v4s*)(p + 4 * 256)));
                    fr[8 + t] = v4u{lo.x, lo.y, hi.x, hi.y};
                }
            }
            __builtin_amdgcn_sched_barrier(0);
            f32x4 d = {0.f, 0.f, 0.f, 0.f}, e = {0.f, 0.f, 0.f, 0.f};
#pragma unroll
            for (int ks = 0; ks < 4; ++ks) { d = mfma(qf[2 * ks], fr[ks], d); e = mfma(qf[2 * ks + 1], fr[4 + ks], e); }
            const unsigned sp = cvtpk(d[0], e[1]);
            const f32x2 s0 = unpk(sp) * 0.08838834764831845f;
            const f32x2 sc = unpk(cvtpk(s0.x, s0.y));
            float mx;
            asm("v_max_f32 %0, %1, %2" : "=v"(mx) : "v"(sc.x), "v"(sc.y));
            const float m = row16_max(mx) * 1.4426950408889634f;
            const float p0 = __builtin_amdgcn_exp2f(__builtin_fmaf(sc.x, 1.4426950408889634f, -m));
            const float p1 = __builtin_amdgcn_exp2f(__builtin_fmaf(sc.y, 1.4426950408889634f, -m));
            const unsigned ph = cvtpk(p0, p1);
            const unsigned pl = cvtpk(p0 - bflo(ph), p1 - bfhi(ph));
            LAS unsigned char* pw = L + L_PL + wv4 * 512 + (qd >> 1) * 256 + (qd & 1) * 64 + c16 * 2;
            *(LAS unsigned short*)pw = (unsigned short)ph;
            *(LAS unsigned short*)(pw + 32) = (unsigned short)(ph >> 16);
            *(LAS unsigned short*)(pw + 128) = (unsigned short)pl;
            *(LAS unsigned short*)(pw + 160) = (unsigned short)(pl >> 16);
            const v4u pf = *(const LAS v4u*)(L + L_PL + wv4 * 512 + (c16 & 7) * 64 + 16 * qd);
            const float lsum = row16_sum(p0 + p1);
            f32x4 o[8];
#pragma unroll
            for (int t = 0; t < 8; ++t) { const f32x4 z = {0.f, 0.f, 0.f, 0.f}; o[t] = mfma(pf, fr[8 + t], z); }
            __builtin_amdgcn_sched_barrier(0);
            LAS unsigned char* ow = L + L_OL + wv4 * 2048 + (qd & 1) * 1024 + c16 * 8;
#pragma unroll
            for (int t = 0; t < 8; ++t) *(LAS f32x2*)(ow + 128 * t) = f32x2{o[t][0], o[t][1]} + f32x2{o[t][2], o[t][3]};
            *(LAS f32x2*)(L + L_ML + (qd * 4 + wv4) * 8) = f32x2{m, lsum};
            STAMP(23 + layer);
        }
        __syncthreads();
        if (layer == 1 || *(const LAS unsigned*)(L + L_TL + 128) != 0u) break;
        __syncthreads();
        }
        {
            const int h = wave >> 1, dim = tid & 127;
            const f32x2 ml = *(const LAS f32x2*)(L + L_ML + (h * 4 + (lane & 3)) * 8);
            float ov[4];
#pragma unroll
            for (int w = 0; w < 4; ++w) ov[w] = *(const LAS float*)(L + L_OL + w * 2048 + (h >> 1) * 1024 + dim * 8 + (h & 1) * 4);
            float M = ml.x;
            MAX_DPP(M, "row_ror:2"); MAX_DPP(M, "row_ror:1");
            const float f = __builtin_amdgcn_exp2f(ml.x - M);
            float o = 0.f;
#pragma unroll
            for (int w = 0; w < 4; ++w) o = __builtin_fmaf(__builtin_bit_cast(float, __builtin_amdgcn_readlane(__builtin_bit_cast(int, f), w)), ov[w], o);
            const unsigned slot = (unsigned)((g * 2 + (h >> 1)) * 32 + i);
            __builtin_amdgcn_raw_buffer_store_b32(canon(o), rs, WS_OP + layer * OP_L + slot * 1024 + (tid & 255) * 4, 0, AUX_SC1);
            float Ls = f * ml.y;
            Ls += dpp<ROR2>(Ls); Ls += dpp<ROR1>(Ls);
            if (dim == 0) __builtin_amdgcn_raw_buffer_store_b64(v2u{canon(M), canon(Ls)}, rs, WS_ML + layer * ML_L + slot * 16 + (h & 1) * 8, 0, AUX_SC1);
        }
        STAMP(5 + 8 * layer);
        if (layer == 0) __builtin_amdgcn_s_sleep(D_E2A); else __builtin_amdgcn_s_sleep(D_E2);
        {
            const int sp16 = lane >> 2, q4 = lane & 3;
            const unsigned ob = WS_OP + layer * OP_L + (team * 32 + sp16) * 1024 + (32 * wave + 4 * q4) * 4;
            const unsigned mb = WS_ML + layer * ML_L + (team * 32 + sp16) * 16 + (wave >> 2) * 8;
            v4u oa0, oa1, ob0, ob1; v2u mla, mlb;
#pragma unroll 1
            for (unsigned sp = 0; sp < SPIN_CAP; ++sp) {
                FENCE();
                oa0 = __builtin_amdgcn_raw_buffer_load_b128(rs, ob, 0, AUX_SC1);
                oa1 = __builtin_amdgcn_raw_buffer_load_b128(rs, ob + 64, 0, AUX_SC1);
                ob0 = __builtin_amdgcn_raw_buffer_load_b128(rs, ob + 16384, 0, AUX_SC1);
                ob1 = __builtin_amdgcn_raw_buffer_load_b128(rs, ob + 16384 + 64, 0, AUX_SC1);
                mla = __builtin_amdgcn_raw_buffer_load_b64(rs, mb, 0, AUX_SC1);
                mlb = __builtin_amdgcn_raw_buffer_load_b64(rs, mb + 256, 0, AUX_SC1);
                const unsigned mx = max(max(umax3(umax4(oa0), umax4(oa1), mla.x), umax3(umax4(ob0), umax4(ob1), mla.y)), max(mlb.x, mlb.y));
                if (__all(mx != SENT)) break;
                __builtin_amdgcn_s_sleep(2);
            }
            STAMP(6 + 8 * layer);
            const float ma = __uint_as_float(mla.x), mbv = __uint_as_float(mlb.x);
            float M;
            asm("v_max_f32 %0, %1, %2" : "=v"(M) : "v"(ma), "v"(mbv));
            MAX_DPP(M, "row_ror:8"); MAX_DPP(M, "row_ror:4");
            M = swap32_max(swap16_max(M));
            const float fa = __builtin_amdgcn_exp2f(ma - M), fb = __builtin_amdgcn_exp2f(mbv - M);
            float Ls = __builtin_fmaf(fa, __uint_as_float(mla.y), fb * __uint_as_float(mlb.y));
            Ls += dpp<ROR8>(Ls); Ls += dpp<ROR4>(Ls);
            Ls = swap16_sum(Ls, Ls); Ls = swap32_sum(Ls, Ls);
            const f32x4 a = __builtin_bit_cast(f32x4, oa0) * fa + __builtin_bit_cast(f32x4, ob0) * fb;
            const f32x4 c = __builtin_bit_cast(f32x4, oa1) * fa + __builtin_bit_cast(f32x4, ob1) * fb;
            float v[4];
#pragma unroll
            for (int k = 0; k < 4; ++k) v[k] = swap32_sum(a[k], c[k]);
            float w0 = swap16_sum(v[0], v[2]), w1v = swap16_sum(v[1], v[3]);
            w0 += dpp<ROR8>(w0); w1v += dpp<ROR8>(w1v);
            w0 += dpp<ROR4>(w0); w1v += dpp<ROR4>(w1v);
            const float rl = __builtin_amdgcn_rcpf(Ls);
            if ((lane & 12) == 0) *(LAS unsigned*)(L + L_OB + (32 * wave + 16 * (qd >> 1) + 4 * q4 + 2 * (qd & 1)) * 2) = cvtpk(w0 * rl, w1v * rl);
        }
        if (layer == 0) {
            *(LAS v4u*)(L + L_WO + (32 * wave + (lane >> 2)) * 80 + 16 * (lane & 3)) = st0;
            *(LAS v4u*)(L + L_WO + (32 * wave + 16 + (lane >> 2)) * 80 + 16 * (lane & 3)) = st1;
        }
        __syncthreads();
        if (wave < 2) {
            if (layer == 0) {
#pragma unroll
                for (int ks = 0; ks < 8; ++ks) {
                    const LAS unsigned char* p = L + L_WO + (32 * ks + 8 * qd + (c16 >> 2)) * 80 + (16 * wave + 4 * (c16 & 3)) * 2;
                    const v2u lo = __builtin_bit_cast(v2u, __builtin_amdgcn_ds_read_tr16_b64_v4i16((LAS v4s*)p));
                    const v2u hi = __builtin_bit_cast(v2u, __builtin_amdgcn_ds_read_tr16_b64_v4i16((LAS v4s*)(p + 4 * 80)));
                    fr[4 + ks] = v4u{lo.x, lo.y, hi.x, hi.y};
                }
            }
            v4u of[8];
#pragma unroll
            for (int ks = 0; ks < 8; ++ks) of[ks] = *(const LAS v4u*)(L + L_OB + 64 * ks + 16 * qd);
            __builtin_amdgcn_sched_barrier(0);
            f32x4 d = {0.f, 0.f, 0.f, 0.f}, e = {0.f, 0.f, 0.f, 0.f};
#pragma unroll
            for (int ks = 0; ks < 8; ks += 2) { d = mfma(of[ks], fr[4 + ks], d); e = mfma(of[ks + 1], fr[5 + ks], e); }
            d[0] += e[0];
            if (lane < 16) __builtin_amdgcn_raw_buffer_store_b32(canon(d[0]), rs, WS_XP + layer * XP_L + (team * 512 + 32 * j + 16 * wave + lane) * 4, 0, AUX_SC1);
        }
        STAMP(7 + 8 * layer);
        if (layer == 1 && b != 0) break;
        if (layer == 0) {
            __builtin_amdgcn_s_sleep(D_E3A);
            v4u tv;
#pragma unroll 1
            for (unsigned sp = 0; sp < SPIN_CAP; ++sp) {
                FENCE();
                tv = __builtin_amdgcn_raw_buffer_load_b128(rs, WS_TAB + tid * 16, 0, AUX_SC1);
                const unsigned mx = max(umax4(tv), umax4(tv << 16));
                if (__all(mx < 0xFFFF0000u)) break;
                __builtin_amdgcn_s_sleep(2);
            }
            __syncthreads();
            *(LAS v4u*)(L + (tid < 256 ? L_TABP : L_TABN - 4096) + tid * 16) = tv;
        } else {
            __builtin_amdgcn_s_sleep(D_E3);
        }
        {
            const unsigned xb = WS_XP + layer * XP_L + (4 * qd * 512 + 64 * wave + 4 * c16) * 4;
            v4u xv[4];
#pragma unroll 1
            for (unsigned sp = 0; sp < SPIN_CAP; ++sp) {
                FENCE();
#pragma unroll
                for (int k = 0; k < 4; ++k) xv[k] = __builtin_amdgcn_raw_buffer_load_b128(rs, xb + k * 2048, 0, AUX_SC1);
                const unsigned mx = max(max(umax4(xv[0]), umax4(xv[1])), max(umax4(xv[2]), umax4(xv[3])));
                if (__all(mx != SENT)) break;
                __builtin_amdgcn_s_sleep(2);
            }
            const f32x4 v = (__builtin_bit_cast(f32x4, xv[0]) + __builtin_bit_cast(f32x4, xv[1])) + (__builtin_bit_cast(f32x4, xv[2]) + __builtin_bit_cast(f32x4, xv[3]));
            const float a0 = swap32_sum(v[0], v[2]), a1 = swap32_sum(v[1], v[3]);
            const float a = swap16_sum(a0, a1);
            *(LAS float*)(L + L_AL + (64 * wave + 4 * c16 + qd) * 4) = rbf(a);
        }
        __syncthreads();
        STAMP(8 + 8 * layer);
        if (wave < 4) {
            const f32x4 a0 = *(const LAS f32x4*)(L + L_AL + 32 * lg), a1 = *(const LAS f32x4*)(L + L_AL + 32 * lg + 16);
            { f32x2 t = unpk(xp.x) + f32x2{a0[0], a0[1]}; xp.x = cvtpk(t.x, t.y); }
            { f32x2 t = unpk(xp.y) + f32x2{a0[2], a0[3]}; xp.y = cvtpk(t.x, t.y); }
            { f32x2 t = unpk(xp.z) + f32x2{a1[0], a1[1]}; xp.z = cvtpk(t.x, t.y); }
            { f32x2 t = unpk(xp.w) + f32x2{a1[2], a1[3]}; xp.w = cvtpk(t.x, t.y); }
            const v4u h2 = layernorm_pk(layernorm_pk(xp));
            unsigned yv[8], big = 0;
#pragma unroll
            for (int k = 0; k < 4; ++k) {
                yv[2 * k] = *(const LAS unsigned short*)(L + ((h2[k] & 0xFFFFu) << 1));
                yv[2 * k + 1] = *(const LAS unsigned short*)(L + ((h2[k] >> 16) << 1));
                big |= ((h2[k] & 0x7FFF7FFFu) + 0x47004700u) ^ 0x80008000u;
            }
            big &= 0xF800F800u;
            if (__any(big != 0u)) {
#pragma unroll 1
                for (int k = 0; k < 8; ++k) {
                    unsigned hw = h2[0];
#pragma unroll
                    for (int kk = 1; kk < 4; ++kk) hw = ((k >> 1) == kk) ? h2[kk] : hw;
                    const unsigned u = (k & 1) ? (hw >> 16) : (hw & 0xFFFFu);
                    unsigned long long mk = __ballot(((u & 0x7FFFu) - (unsigned)(TAB_E0 << 7)) >= 2048u);
#pragma unroll 1
                    while (mk) {
                        const int ln = __builtin_ctzll(mk);
                        mk &= mk - 1;
                        const float tv = __uint_as_float((unsigned)__builtin_amdgcn_readlane((int)u, ln) << 16);
                        float a = 0.f;
#pragma unroll 1
                        for (int p = lane; p < FF / 2; p += 64) {
                            const unsigned ww1 = *(const unsigned*)(w1 + 2 * p), bb1 = *(const unsigned*)(b1 + 2 * p), ww2 = *(const unsigned*)(w2 + 2 * p);
                            a = mlp_pair(tv, unpk(ww1), unpk(bb1), ww2, a);
                        }
                        const unsigned y = cvtpk(rbf(wave_sum(a)) + b2f, 0.f) & 0xFFFFu;
#pragma unroll
                        for (int kk = 0; kk < 8; ++kk) yv[kk] = (k == kk && lane == ln) ? y : yv[kk];
                    }
                }
            }
#pragma unroll
            for (int k = 0; k < 4; ++k) {
                const f32x2 t = unpk(xp[k]) + f32x2{__uint_as_float(yv[2 * k] << 16), __uint_as_float(yv[2 * k + 1] << 16)};
                xp[k] = cvtpk(t.x, t.y);
            }
            if (layer == 0) {
                stage_q(layernorm_pk(xp), 1);
                STAMP(9);
            } else if (wave == 0) {
                *(v4u*)(out + 8 * lg) = xp;
            }
        }
    }
    STAMP(17);
    STAMP_DUMP();
}

extern "C" void kernel_launch(void* const* d_in, const int* in_sizes, int n_in, void* d_out, int out_size, void* d_ws,
                              size_t ws_size, hipStream_t stream) {
    const bf16_t* x = (const bf16_t*)d_in[0];
    const bf16_t* kc = (const bf16_t*)d_in[1];
    const bf16_t* vc = (const bf16_t*)d_in[2];
    const bf16_t* wq = (const bf16_t*)d_in[3];
    const bf16_t* wo = (const bf16_t*)d_in[4];
    const bf16_t* w1 = (const bf16_t*)d_in[6];
    const bf16_t* b1 = (const bf16_t*)d_in[7];
    const bf16_t* w2 = (const bf16_t*)d_in[8];
    const bf16_t* b2 = (const bf16_t*)d_in[9];
    (void)hipFuncSetAttribute((const void*)fused_kernel, hipFuncAttributeMaxDynamicSharedMemorySize, LDS_BYTES);
    fused_kernel<<<NWG, NTHR, LDS_BYTES, stream>>>(x, kc, vc, wq, wo, w1, b1, w2, b2, (bf16_t*)d_out, d_ws);
}
```

```cpp
#include <hip/hip_runtime.h>
#include <hip/hip_bf16.h>
#include <stdint.h>

typedef unsigned short bf16_t;
typedef unsigned v4u __attribute__((ext_vector_type(4)));
typedef unsigned v2u __attribute__((ext_vector_type(2)));
typedef float f32x4 __attribute__((ext_vector_type(4)));
typedef float f32x2 __attribute__((ext_vector_type(2)));
typedef short bf16x8 __attribute__((ext_vector_type(8)));
typedef short v4s __attribute__((ext_vector_type(4)));
typedef __bf16 bf16x2 __attribute__((ext_vector_type(2)));
#define LAS __attribute__((address_space(3)))

#define D_E1 10
#define D_E2 12
#define D_E3 18
#define D_K0 12
#define D_FL 70
#define D_Q0 8
#define TABK 7
#define KB1 31
#define AUX_MW 0
#define INFL 32
#define S1_STEP 2
#define WO_STEP 3
#define D_V0 0
#define D_FR 8
#define CODE_PF 20480
#define D_E2A 20
#define D_E2X 6
#define D_E3A 8

constexpr int DM = 512, SK = 65536, WIN = 4096, FF = 1792;
constexpr int NWG = 256, NTHR = 640;
constexpr unsigned SENT = 0xFFFFFFFFu;
constexpr int AUX_SC1 = 16, AUX_NT = 2;
constexpr unsigned SPIN_CAP = 1u << 20;

constexpr unsigned WS_EPOCH = 0;
constexpr unsigned WS_FLAGS = 4096;
constexpr unsigned WS_TAB = 8192;
constexpr unsigned WS_QP = 16384;
constexpr unsigned QP_L = 8 * 4 * 512 * 4;
constexpr unsigned WS_OP = WS_QP + 2 * QP_L;
constexpr unsigned OP_L = 8 * 2 * 32 * 256 * 4;
constexpr unsigned WS_ML = WS_OP + 2 * OP_L;
constexpr unsigned ML_L = 8 * 2 * 32 * 4 * 4;
constexpr unsigned WS_XP = WS_ML + 2 * ML_L;
constexpr unsigned XP_L = 16 * 512 * 4;
constexpr unsigned WS_END = WS_XP + 2 * XP_L;
constexpr unsigned WS_STAMP = 4u << 20;

constexpr int L_K = 0;
constexpr int L_V = 32768;
constexpr int L_WQ = 65536;
constexpr int L_WO = L_WQ + 16384;
constexpr int L_OL = L_WO + 256 * 80;
constexpr int L_PL = L_OL + 8192;
constexpr int L_AL = L_PL + 2048;
constexpr int L_OB = L_AL + 2048;
constexpr int L_ML = L_OB + 512;
constexpr int L_QS = L_ML + 128;
constexpr int L_TL = L_QS + 1280;
constexpr int L_END = L_TL + 128 + 16;
constexpr int LDS_BYTES = 144 * 1024;
static_assert(L_END + 2560 <= LDS_BYTES, "lds");
static_assert(L_QS % 16 == 0 && L_TL % 16 == 0 && L_OL % 16 == 0, "align");
constexpr int L_TABP = 2 * (114 << 7), L_TABN = 2 * (0x8000 + (114 << 7));
static_assert(L_TABP + 4096 <= L_WQ && L_TABN >= L_WO && L_TABN + 4096 <= L_OL && 131072 <= LDS_BYTES, "table placement");

constexpr int TAB_E0 = 114;

#define FENCE() asm volatile("" ::: "memory")

__device__ __forceinline__ float bflo(unsigned p) { return __uint_as_float(p << 16); }
__device__ __forceinline__ float bfhi(unsigned p) { return __uint_as_float(p & 0xFFFF0000u); }
__device__ __forceinline__ unsigned cvtpk(float a, float b) {
    f32x2 v = {a, b};
    return __builtin_bit_cast(unsigned, __builtin_convertvector(v, bf16x2));
}
__device__ __forceinline__ float rbf(float a) { return bflo(cvtpk(a, a)); }
__device__ __forceinline__ float dot2(unsigned a, unsigned b, float c) {
    return __builtin_amdgcn_fdot2_f32_bf16(__builtin_bit_cast(bf16x2, a), __builtin_bit_cast(bf16x2, b), c, false);
}
template <int CTRL> __device__ __forceinline__ float dpp(float x) {
    return __builtin_bit_cast(float, __builtin_amdgcn_mov_dpp(__builtin_bit_cast(int, x), CTRL, 0xf, 0xf, true));
}
constexpr int ROR1 = 0x121, ROR2 = 0x122, ROR4 = 0x124, ROR8 = 0x128;
__device__ __forceinline__ float swap32_sum(float a, float b) {
    auto r = __builtin_amdgcn_permlane32_swap(__float_as_uint(a), __float_as_uint(b), false, false);
    return __uint_as_float(r[0]) + __uint_as_float(r[1]);
}
__device__ __forceinline__ float swap16_sum(float a, float b) {
    auto r = __builtin_amdgcn_permlane16_swap(__float_as_uint(a), __float_as_uint(b), false, false);
    return __uint_as_float(r[0]) + __uint_as_float(r[1]);
}
__device__ __forceinline__ float swap32_max(float a) {
    auto r = __builtin_amdgcn_permlane32_swap(__float_as_uint(a), __float_as_uint(a), false, false);
    float o;
    asm("v_max_f32 %0, %1, %2" : "=v"(o) : "v"(r[0]), "v"(r[1]));
    return o;
}
__device__ __forceinline__ float swap16_max(float a) {
    auto r = __builtin_amdgcn_permlane16_swap(__float_as_uint(a), __float_as_uint(a), false, false);
    float o;
    asm("v_max_f32 %0, %1, %2" : "=v"(o) : "v"(r[0]), "v"(r[1]));
    return o;
}
__device__ __forceinline__ float row16_sum(float v) {
    v += dpp<ROR8>(v); v += dpp<ROR4>(v); v += dpp<ROR2>(v); v += dpp<ROR1>(v);
    return v;
}
#define MAX_DPP(v, CTRL) asm volatile("s_nop 1\n\tv_max_f32_dpp %0, %0, %0 " CTRL " row_mask:0xf bank_mask:0xf" : "+v"(v))
__device__ __forceinline__ float row16_max(float v) {
    MAX_DPP(v, "row_ror:8"); MAX_DPP(v, "row_ror:4"); MAX_DPP(v, "row_ror:2"); MAX_DPP(v, "row_ror:1");
    return v;
}
__device__ __forceinline__ float row8_max(float v) {
    MAX_DPP(v, "row_ror:4"); MAX_DPP(v, "row_ror:2"); MAX_DPP(v, "row_ror:1");
    return v;
}
__device__ __forceinline__ float wave_sum(float v) { v = row16_sum(v); v = swap16_sum(v, v); return swap32_sum(v, v); }
__device__ __forceinline__ unsigned canon(float v) {
    unsigned u = __float_as_uint(v);
    return u < SENT ? u : 0x7FC00000u;
}
__device__ __forceinline__ unsigned umax3(unsigned a, unsigned b, unsigned c) { return max(max(a, b), c); }
__device__ __forceinline__ unsigned umax4(v4u a) { return max(umax3(a.x, a.y, a.z), a.w); }
__device__ __forceinline__ f32x4 mfma(v4u a, v4u b, f32x4 c) {
    return __builtin_amdgcn_mfma_f32_16x16x32_bf16(__builtin_bit_cast(bf16x8, a), __builtin_bit_cast(bf16x8, b), c, 0, 0, 0);
}
__device__ __forceinline__ float mlp_pair(float t, f32x2 w1, f32x2 b1, unsigned w2p, float acc) {
    const f32x2 c1 = {-2.3022081986f, -2.3022081986f}, c2 = {-0.1029432396f, -0.1029432396f};
    const f32x2 tt = {t, t};
    const f32x2 a = tt * w1;
    const unsigned p = cvtpk(a.x, a.y);
    f32x2 u = {bflo(p), bfhi(p)};
    u += b1;
    const unsigned q = cvtpk(u.x, u.y);
    u.x = bflo(q); u.y = bfhi(q);
    const f32x2 z = u * __builtin_elementwise_fma(u * u, c2, c1);
    f32x2 e = {__builtin_amdgcn_exp2f(z.x), __builtin_amdgcn_exp2f(z.y)};
    e += 1.0f;
    const f32x2 r = {__builtin_amdgcn_rcpf(e.x), __builtin_amdgcn_rcpf(e.y)};
    const f32x2 gl = u * r;
    return dot2(cvtpk(gl.x, gl.y), w2p, acc);
}

__device__ __forceinline__ void mlp_pair2(float t0, float t1, f32x2 w1, f32x2 b1, unsigned w2p, float& acc0, float& acc1) {
    const f32x2 c1 = {-2.3022081986f, -2.3022081986f}, c2 = {-0.1029432396f, -0.1029432396f};
    const f32x2 a0 = f32x2{t0, t0} * w1, a1 = f32x2{t1, t1} * w1;
    const unsigned p0 = cvtpk(a0.x, a0.y), p1 = cvtpk(a1.x, a1.y);
    f32x2 u0 = {bflo(p0), bfhi(p0)}, u1 = {bflo(p1), bfhi(p1)};
    u0 += b1; u1 += b1;
    const unsigned q0 = cvtpk(u0.x, u0.y), q1 = cvtpk(u1.x, u1.y);
    u0 = f32x2{bflo(q0), bfhi(q0)}; u1 = f32x2{bflo(q1), bfhi(q1)};
    const f32x2 s0 = u0 * u0, s1 = u1 * u1;
    const f32x2 y0 = __builtin_elementwise_fma(s0, c2, c1), y1 = __builtin_elementwise_fma(s1, c2, c1);
    const f32x2 z0 = u0 * y0, z1 = u1 * y1;
    f32x2 e0 = {__builtin_amdgcn_exp2f(z0.x), __builtin_amdgcn_exp2f(z0.y)}, e1 = {__builtin_amdgcn_exp2f(z1.x), __builtin_amdgcn_exp2f(z1.y)};
    e0 += 1.0f; e1 += 1.0f;
    const f32x2 r0 = {__builtin_amdgcn_rcpf(e0.x), __builtin_amdgcn_rcpf(e0.y)}, r1 = {__builtin_amdgcn_rcpf(e1.x), __builtin_amdgcn_rcpf(e1.y)};
    const f32x2 g0 = u0 * r0, g1 = u1 * r1;
    const unsigned h0 = cvtpk(g0.x, g0.y), h1 = cvtpk(g1.x, g1.y);
    acc0 = dot2(h0, w2p, acc0); acc1 = dot2(h1, w2p, acc1);
}
__device__ __forceinline__ f32x2 unpk(unsigned p) { return f32x2{bflo(p), bfhi(p)}; }
__device__ __forceinline__ v4u layernorm_pk(v4u xp) {
    const unsigned ONES = 0x3F803F80u;
    float s1 = dot2(xp.x, ONES, 0.f), s2 = dot2(xp.x, xp.x, 0.f);
    s1 = dot2(xp.y, ONES, s1); s2 = dot2(xp.y, xp.y, s2);
    s1 = dot2(xp.z, ONES, s1); s2 = dot2(xp.z, xp.z, s2);
    s1 = dot2(xp.w, ONES, s1); s2 = dot2(xp.w, xp.w, s2);
    f32x2 d[4];
#pragma unroll
    for (int k = 0; k < 4; ++k) d[k] = unpk(xp[k]);
    s1 += dpp<ROR8>(s1); s2 += dpp<ROR8>(s2);
    s1 += dpp<ROR4>(s1); s2 += dpp<ROR4>(s2);
    s1 += dpp<ROR2>(s1); s2 += dpp<ROR2>(s2);
    s1 += dpp<ROR1>(s1); s2 += dpp<ROR1>(s2);
    float t = swap16_sum(s1, s2);
    t = swap32_sum(t, t);
    auto r2 = __builtin_amdgcn_permlane16_swap(__float_as_uint(t), __float_as_uint(t), false, false);
    const float mean = __uint_as_float(r2[0]) * (1.0f / DM);
    const float msq = mean * mean;
    float var = __builtin_fmaf(__uint_as_float(r2[1]), 1.0f / DM, -msq);
    const float m = rbf(mean);
    const f32x2 mm = {m, m};
#pragma unroll
    for (int k = 0; k < 4; ++k) d[k] -= mm;
    if (__builtin_expect(!(msq <= 64.f * var), 0)) {
        f32x2 q = d[0] * d[0];
#pragma unroll
        for (int k = 1; k < 4; ++k) q = __builtin_elementwise_fma(d[k], d[k], q);
        const float dm = mean - m;
        var = fmaxf(__builtin_fmaf(-dm, dm, wave_sum(q.x + q.y) * (1.0f / DM)), 0.f);
    }
    const float sd = rbf(__builtin_amdgcn_sqrtf(rbf(rbf(var) + 1e-5f)));
    const float r = __builtin_amdgcn_rcpf(sd);
    const f32x2 rr = {r, r};
    v4u o;
#pragma unroll
    for (int k = 0; k < 4; ++k) {
        const f32x2 tt = unpk(cvtpk(d[k].x, d[k].y)) * rr;
        o[k] = cvtpk(tt.x, tt.y);
    }
    return o;
}

#define STAMP(n) do { } while (0)
#define STAMP_DUMP() do { } while (0)
#define WAITVM(n) do { } while (0)

__global__ void __launch_bounds__(NTHR, 3)
fused_kernel(const bf16_t* x, const bf16_t* kc, const bf16_t* vc, const bf16_t* wq, const bf16_t* wo, const bf16_t* w1,
             const bf16_t* b1, const bf16_t* w2, const bf16_t* b2, bf16_t* out, void* ws) {
    extern __shared__ __attribute__((aligned(16))) unsigned char smem[];
    LAS unsigned char* L = (LAS unsigned char*)smem;
    const int b = blockIdx.x, tid = threadIdx.x, lane = tid & 63;
    const int wave = __builtin_amdgcn_readfirstlane(tid >> 6);
    const int g = b & 7, i = b >> 3, hp = i & 1, j = i >> 1, cb = i & 7, rb = i >> 3;
    const __amdgpu_buffer_rsrc_t rs = __builtin_amdgcn_make_buffer_rsrc(ws, 0, (int)WS_END, 0x00020000);
    const unsigned team = (unsigned)(g * 2 + hp);
    STAMP(0);

    auto gather_q = [&](int layer) {
        const unsigned base = WS_QP + layer * QP_L + g * 8192 + lane * 16;
        v4u qv[8];
#pragma unroll 1
        for (unsigned sp = 0; sp < SPIN_CAP; ++sp) {
            FENCE();
#pragma unroll
            for (int k = 0; k < 8; ++k) qv[k] = __builtin_amdgcn_raw_buffer_load_b128(rs, base + k * 1024, 0, AUX_SC1);
            unsigned mx = 0;
#pragma unroll
            for (int k = 0; k < 8; ++k) mx = max(mx, umax4(qv[k]));
            if (__all(mx != SENT)) break;
            __builtin_amdgcn_s_sleep(2);
        }
#pragma unroll
        for (int hf = 0; hf < 2; ++hf) {
            const f32x4 q = (__builtin_bit_cast(f32x4, qv[hf]) + __builtin_bit_cast(f32x4, qv[2 + hf])) + (__builtin_bit_cast(f32x4, qv[4 + hf]) + __builtin_bit_cast(f32x4, qv[6 + hf]));
            *(LAS v2u*)(L + L_QS + hf * 512 + lane * 8) = v2u{cvtpk(q[0], q[1]), cvtpk(q[2], q[3])};
        }
    };

    if (wave == 9) {
        __builtin_amdgcn_s_setprio(3);
        if (i < 4) {
            unsigned long long pc;
            asm volatile("s_getpc_b64 %0" : "=s"(pc));
            const v4u* cp = (const v4u*)((pc & ~255ull) + (unsigned long long)i * (CODE_PF / 4)) + lane;
            v4u acc = {0u, 0u, 0u, 0u};
#pragma unroll
            for (int k = 0; k < CODE_PF / 4096; ++k) acc |= cp[64 * k];
            asm volatile("" :: "v"(acc));
        }
        const size_t kvb = ((size_t)g * SK + (SK - WIN) + 128 * i) * 128;
        const __amdgpu_buffer_rsrc_t rk = __builtin_amdgcn_make_buffer_rsrc((void*)(kc + kvb), 0, 32768, 0x00020000);
        const __amdgpu_buffer_rsrc_t rv = __builtin_amdgcn_make_buffer_rsrc((void*)(vc + kvb), 0, 32768, 0x00020000);
        int ko[4], vo[4];
#pragma unroll
        for (int k = 0; k < 4; ++k) {
            const int row = 4 * k + (lane >> 4);
            ko[k] = row * 256 + 16 * ((lane & 15) ^ row);
            vo[k] = row * 256 + 16 * ((lane & 15) ^ (2 * ((row & 3) | ((row >> 3) << 2))));
        }
        __builtin_amdgcn_s_barrier();
        __builtin_amdgcn_s_sleep(D_K0);
#pragma unroll
        for (int it = 0; it < 64; ++it) {
            if (it < 32) __builtin_amdgcn_raw_ptr_buffer_load_lds(rk, (LAS unsigned*)(L + L_K + it * 1024), 16, ko[it & 3], (it >> 2) * 4096, 0, AUX_NT);
            else __builtin_amdgcn_raw_ptr_buffer_load_lds(rv, (LAS unsigned*)(L + L_V + (it - 32) * 1024), 16, vo[it & 3], ((it - 32) >> 2) * 4096, 0, AUX_NT);
            if (it == KB1) { __builtin_amdgcn_s_barrier(); __builtin_amdgcn_s_sleep(D_V0); }
        }
        asm volatile("s_waitcnt vmcnt(0)" ::: "memory");
        STAMP(20);
        __builtin_amdgcn_s_barrier();
        STAMP_DUMP();
        return;
    }
    if (wave == 8) {
        __builtin_amdgcn_s_setprio(3);
        const v2u ep = __builtin_amdgcn_raw_buffer_load_b64(rs, WS_EPOCH, 0, AUX_SC1);
        const v4u sv = {SENT, SENT, SENT, SENT};
#pragma unroll
        for (int l2 = 0; l2 < 2; ++l2) {
            __builtin_amdgcn_raw_buffer_store_b128(sv, rs, WS_OP + l2 * OP_L + ((g * 2) * 32 + i) * 1024 + lane * 16, 0, AUX_SC1);
            __builtin_amdgcn_raw_buffer_store_b128(sv, rs, WS_OP + l2 * OP_L + ((g * 2 + 1) * 32 + i) * 1024 + lane * 16, 0, AUX_SC1);
            unsigned off;
            if (lane < 16) off = WS_QP + l2 * QP_L + ((g * 4 + rb) * 512 + cb * 64) * 4 + lane * 16;
            else if (lane < 24) off = WS_XP + l2 * XP_L + (team * 512 + j * 32) * 4 + (lane - 16) * 16;
            else if (lane < 26) off = WS_ML + l2 * ML_L + ((g * 2 + (lane - 24)) * 32 + i) * 16;
            else off = WS_TAB + b * 32 + (lane & 1) * 16;
            if (lane < 28) __builtin_amdgcn_raw_buffer_store_b128(sv, rs, off, 0, AUX_SC1);
        }
        *(LAS unsigned*)(L + L_QS + 1024 + lane * 4) = 0u;
        FENCE();
        __builtin_amdgcn_s_barrier();
        FENCE();
        const unsigned long long T = (((unsigned long long)ep.y << 32) | ep.x) + 0x9E3779B97F4A7C15ull;
        const unsigned tlo = __builtin_amdgcn_readfirstlane((unsigned)T), thi = __builtin_amdgcn_readfirstlane((unsigned)(T >> 32));
        asm volatile("s_waitcnt vmcnt(0)" ::: "memory");
        STAMP(20);
        if (lane == 0) __builtin_amdgcn_raw_buffer_store_b64(v2u{tlo, thi}, rs, WS_FLAGS + b * 8, 0, AUX_SC1);
        __syncthreads();
        __builtin_amdgcn_s_sleep(D_FL);
        const unsigned qbase = WS_QP + g * 8192 + lane * 16;
        v4u qv[8];
#pragma unroll 1
        for (unsigned sp = 0; sp < SPIN_CAP; ++sp) {
            FENCE();
            const v4u f0 = __builtin_amdgcn_raw_buffer_load_b128(rs, WS_FLAGS + lane * 32, 0, AUX_SC1);
            const v4u f1 = __builtin_amdgcn_raw_buffer_load_b128(rs, WS_FLAGS + lane * 32 + 16, 0, AUX_SC1);
#pragma unroll
            for (int k = 0; k < 8; ++k) qv[k] = __builtin_amdgcn_raw_buffer_load_b128(rs, qbase + k * 1024, 0, AUX_SC1);
            const unsigned bad = (f0.x ^ tlo) | (f0.y ^ thi) | (f0.z ^ tlo) | (f0.w ^ thi) | (f1.x ^ tlo) | (f1.y ^ thi) | (f1.z ^ tlo) | (f1.w ^ thi);
            unsigned mx = 0;
#pragma unroll
            for (int k = 0; k < 8; ++k) mx = max(mx, umax4(qv[k]));
            if (__all(bad == 0u && mx != SENT)) break;
            __builtin_amdgcn_s_sleep(D_FR);
        }
        if (b == 0 && lane == 0) __builtin_amdgcn_raw_buffer_store_b64(v2u{tlo, thi}, rs, WS_EPOCH, 0, AUX_SC1);
        STAMP(21);
#pragma unroll 1
        for (unsigned sp = 0; sp < SPIN_CAP; ++sp) {
            FENCE();
            v4u rv2[8];
#pragma unroll
            for (int k = 0; k < 8; ++k) rv2[k] = __builtin_amdgcn_raw_buffer_load_b128(rs, qbase + k * 1024, 0, AUX_SC1);
            FENCE();
#pragma unroll
            for (int hf = 0; hf < 2; ++hf) {
                const f32x4 q = (__builtin_bit_cast(f32x4, qv[hf]) + __builtin_bit_cast(f32x4, qv[2 + hf])) + (__builtin_bit_cast(f32x4, qv[4 + hf]) + __builtin_bit_cast(f32x4, qv[6 + hf]));
                *(LAS v2u*)(L + L_QS + hf * 512 + lane * 8) = v2u{cvtpk(q[0], q[1]), cvtpk(q[2], q[3])};
            }
            asm volatile("s_waitcnt lgkmcnt(0)" ::: "memory");
            __builtin_amdgcn_s_barrier();
            FENCE();
            unsigned diff = 0;
#pragma unroll
            for (int k = 0; k < 8; ++k) { const v4u d = rv2[k] ^ qv[k]; diff |= d.x | d.y | d.z | d.w; qv[k] = rv2[k]; }
            const bool same = __all(diff == 0u);
            if (!same) {
#pragma unroll 1
                for (unsigned s2 = 0; s2 < SPIN_CAP; ++s2) {
                    unsigned mx = 0;
#pragma unroll
                    for (int k = 0; k < 8; ++k) mx = max(mx, umax4(qv[k]));
                    if (__all(mx != SENT)) break;
                    __builtin_amdgcn_s_sleep(4);
                    FENCE();
#pragma unroll
                    for (int k = 0; k < 8; ++k) qv[k] = __builtin_amdgcn_raw_buffer_load_b128(rs, qbase + k * 1024, 0, AUX_SC1);
                }
            }
            if (lane == 0) *(LAS unsigned*)(L + L_TL + 128) = same ? 1u : 0u;
            __syncthreads();
            if (same) break;
        }
        STAMP(22);
        STAMP_DUMP();
        return;
    }

    const int qd = lane >> 4, c16 = lane & 15, wv4 = wave & 3;
    const int lg = 16 * (c16 >> 2) + 4 * (c16 & 3) + qd;
    const int pg = tid & 127, eg = tid >> 7;
    unsigned w1p[7], b1p[7], w2p[7];
#pragma unroll
    for (int k = 0; k < 7; ++k) {
        w1p[k] = *(const unsigned*)(w1 + 2 * (pg + 128 * k));
        b1p[k] = *(const unsigned*)(b1 + 2 * (pg + 128 * k));
        w2p[k] = *(const unsigned*)(w2 + 2 * (pg + 128 * k));
    }
    const float b2f = __uint_as_float((unsigned)b2[0] << 16);
    const v4u xin = *(const v4u*)(x + 8 * lg);
    FENCE();
    const __amdgpu_buffer_rsrc_t rwq = __builtin_amdgcn_make_buffer_rsrc((void*)(wq + (size_t)(128 * rb) * 4096 + 512 * g + 64 * cb), 0, 128 * 8192, 0x00020000);
    v4u st0, st1;
    st0 = __builtin_amdgcn_raw_buffer_load_b128(rwq, (16 * wave + (lane >> 3)) * 8192 + 16 * (lane & 7), 0, AUX_NT);
    st1 = __builtin_amdgcn_raw_buffer_load_b128(rwq, (16 * wave + (lane >> 3) + 8) * 8192 + 16 * (lane & 7), 0, AUX_NT);
    FENCE();
    __builtin_amdgcn_s_barrier();
    FENCE();
    v4u xp = xin;
    v4u fr[16];
    const unsigned qp_off = WS_QP + ((g * 4 + rb) * 512 + cb * 64 + 16 * wv4 + c16) * 4;
    auto stage_q = [&](v4u h, int layer) {
        const f32x4 z = {0.f, 0.f, 0.f, 0.f};
        const f32x4 d0 = mfma(h, fr[0], z), d1 = mfma(h, fr[1], z), d2 = mfma(h, fr[2], z), d3 = mfma(h, fr[3], z);
        const float q = (d0[0] + d1[1]) + (d2[2] + d3[3]);
        if (qd == rb) __builtin_amdgcn_raw_buffer_store_b32(canon(q), rs, qp_off + layer * QP_L, 0, AUX_SC1);
    };

    v4u hn0 = {0u, 0u, 0u, 0u};
    if (wave < 4) hn0 = layernorm_pk(xp);
    {
        f32x2 w1f[7], b1f[7];
#pragma unroll
        for (int k = 0; k < 7; ++k) { w1f[k] = unpk(w1p[k]); b1f[k] = unpk(b1p[k]); }
        float acc[4] = {0.f, 0.f, 0.f, 0.f};
#pragma unroll
        for (int u = 0; u < 4; u += 2) {
            const unsigned idx = (unsigned)(16 * b + 4 * eg + u);
            const float tv0 = __uint_as_float((((idx >> 11) << 15) | ((idx & 0x7FFu) + (TAB_E0 << 7))) << 16);
            const float tv1 = __uint_as_float(__float_as_uint(tv0) + 0x10000u);
#pragma unroll
            for (int k = 0; k < TABK; ++k) {
                if (u == 0 && k == S1_STEP) {
                    asm volatile("" : "+v"(acc[0]), "+v"(acc[1]) :: "memory");
                    *(LAS v4u*)(L + L_WQ + (16 * wave + (lane >> 3)) * 128 + 16 * (lane & 7)) = st0;
                    *(LAS v4u*)(L + L_WQ + (16 * wave + (lane >> 3) + 8) * 128 + 16 * (lane & 7)) = st1;
                    STAMP(20);
                    __syncthreads();
                    if (wave < 4) {
                        __builtin_amdgcn_s_setprio(2);
#pragma unroll
                        for (int ks = 0; ks < 4; ++ks) {
                            const LAS unsigned char* p = L + L_WQ + (32 * ks + 8 * qd + (c16 >> 2)) * 128 + (16 * wv4 + 4 * (c16 & 3)) * 2;
                            const v2u lo = __builtin_bit_cast(v2u, __builtin_amdgcn_ds_read_tr16_b64_v4i16((LAS v4s*)p));
                            const v2u hi = __builtin_bit_cast(v2u, __builtin_amdgcn_ds_read_tr16_b64_v4i16((LAS v4s*)(p + 4 * 128)));
                            fr[ks] = v4u{lo.x, lo.y, hi.x, hi.y};
                        }
                        stage_q(hn0, 0);
                        __builtin_amdgcn_s_setprio(0);
                    }
                    STAMP(2);
                    FENCE();
                    __builtin_amdgcn_sched_barrier(0);
                }
                mlp_pair2(tv0, tv1, w1f[k], b1f[k], w2p[k], acc[u], acc[u + 1]);
                __builtin_amdgcn_sched_barrier(0);
            }
        }
        acc[0] = swap32_sum(acc[0], acc[2]); acc[1] = swap32_sum(acc[1], acc[3]);
        acc[0] = row16_sum(swap16_sum(acc[0], acc[1]));
        if (c16 == 0) *(LAS float*)(L + L_TL + (wave * 4 + qd) * 4) = acc[0];
    }
    STAMP(25);
    __syncthreads();
    STAMP(3);
    {
        const __amdgpu_buffer_rsrc_t rwo = __builtin_amdgcn_make_buffer_rsrc((void*)(wo + (size_t)(512 * g + 256 * hp) * 512 + 32 * j), 0, 256 * 1024, 0x00020000);
        st0 = __builtin_amdgcn_raw_buffer_load_b128(rwo, (32 * wave + (lane >> 2)) * 1024 + 16 * (lane & 3), 0, AUX_NT);
        st1 = __builtin_amdgcn_raw_buffer_load_b128(rwo, (32 * wave + 16 + (lane >> 2)) * 1024 + 16 * (lane & 3), 0, AUX_NT);
        FENCE();
    }
    if (wave >= 4) {
#pragma unroll
        for (int T = 0; T < 2; ++T)
#pragma unroll
            for (int ks = 0; ks < 4; ++ks)
                fr[4 * T + ks] = *(const LAS v4u*)(L + L_K + (32 * wv4 + 16 * T + c16) * 256 + 16 * ((4 * ks + qd) ^ c16));
    } else if (tid < 16) {
        const int e2 = tid >> 2, u = tid & 3;
        const float sum = *(const LAS float*)(L + L_TL + ((2 * e2) * 4 + u) * 4) + *(const LAS float*)(L + L_TL + ((2 * e2 + 1) * 4 + u) * 4);
        unsigned y = cvtpk(rbf(sum) + b2f, 0.f) & 0xFFFFu;
        if (y == 0xFFFFu) y = 0x7FC0u;
        __builtin_amdgcn_raw_buffer_store_b16((unsigned short)y, rs, WS_TAB + (16 * b + tid) * 2, 0, AUX_SC1);
    }
    const int qa0 = L_QS + ((c16 & 3) == 0 ? (c16 >> 2) * 256 : 1024) + 16 * qd;
    const int qa1 = L_QS + ((c16 & 3) == 1 ? (c16 >> 2) * 256 : 1024) + 16 * qd;

#pragma unroll
    for (int layer = 0; layer < 2; ++layer) {
        if (layer == 1) {
            if (wave == 0) { __builtin_amdgcn_s_sleep(D_E1); gather_q(1); }
            __syncthreads();
        }
        STAMP(4 + 8 * layer);
#pragma unroll 1
        for (unsigned redo = 0; redo < SPIN_CAP; ++redo) {
        if (wave >= 4) {
            v4u qf[8];
#pragma unroll
            for (int ks = 0; ks < 4; ++ks) { qf[2 * ks] = *(const LAS v4u*)(L + qa0 + 64 * ks); qf[2 * ks + 1] = *(const LAS v4u*)(L + qa1 + 64 * ks); }
            if (layer == 0 && redo == 0) {
                const int vrow = 32 * wv4 + 8 * qd + (c16 >> 2);
                const int vsw = 2 * ((vrow & 3) | (((vrow >> 3) & 1) << 2));
                const LAS unsigned char* vb = L + L_V + vrow * 256 + 8 * (c16 & 1);
#pragma unroll
                for (int t = 0; t < 8; ++t) {
                    const LAS unsigned char* p = vb + 16 * ((2 * t + ((c16 >> 1) & 1)) ^ vsw);
                    const v2u lo = __builtin_bit_cast(v2u, __builtin_amdgcn_ds_read_tr16_b64_v4i16((LAS v4s*)p));
                    const v2u hi = __builtin_bit_cast(v2u, __builtin_amdgcn_ds_read_tr16_b64_v4i16((LAS v4s*)(p + 4 * 256)));
                    fr[8 + t] = v4u{lo.x, lo.y, hi.x, hi.y};
                }
            }
            __builtin_amdgcn_sched_barrier(0);
            f32x4 d = {0.f, 0.f, 0.f, 0.f}, e = {0.f, 0.f, 0.f, 0.f};
#pragma unroll
            for (int ks = 0; ks < 4; ++ks) { d = mfma(qf[2 * ks], fr[ks], d); e = mfma(qf[2 * ks + 1], fr[4 + ks], e); }
            const unsigned sp = cvtpk(d[0], e[1]);
            const f32x2 s0 = unpk(sp) * 0.08838834764831845f;
            const f32x2 sc = unpk(cvtpk(s0.x, s0.y));
            float mx;
            asm("v_max_f32 %0, %1, %2" : "=v"(mx) : "v"(sc.x), "v"(sc.y));
            const float m = row16_max(mx) * 1.4426950408889634f;
            const float p0 = __builtin_amdgcn_exp2f(__builtin_fmaf(sc.x, 1.4426950408889634f, -m));
            const float p1 = __builtin_amdgcn_exp2f(__builtin_fmaf(sc.y, 1.4426950408889634f, -m));
            const unsigned ph = cvtpk(p0, p1);
            const unsigned pl = cvtpk(p0 - bflo(ph), p1 - bfhi(ph));
            LAS unsigned char* pw = L + L_PL + wv4 * 512 + (qd >> 1) * 256 + (qd & 1) * 64 + c16 * 2;
            *(LAS unsigned short*)pw = (unsigned short)ph;
            *(LAS unsigned short*)(pw + 32) = (unsigned short)(ph >> 16);
            *(LAS unsigned short*)(pw + 128) = (unsigned short)pl;
            *(LAS unsigned short*)(pw + 160) = (unsigned short)(pl >> 16);
            const v4u pf = *(const LAS v4u*)(L + L_PL + wv4 * 512 + (c16 & 7) * 64 + 16 * qd);
            const float lsum = row16_sum(p0 + p1);
            f32x4 o[8];
#pragma unroll
            for (int t = 0; t < 8; ++t) { const f32x4 z = {0.f, 0.f, 0.f, 0.f}; o[t] = mfma(pf, fr[8 + t], z); }
            __builtin_amdgcn_sched_barrier(0);
            LAS unsigned char* ow = L + L_OL + wv4 * 2048 + (qd & 1) * 1024 + c16 * 8;
#pragma unroll
            for (int t = 0; t < 8; ++t) *(LAS f32x2*)(ow + 128 * t) = f32x2{o[t][0], o[t][1]} + f32x2{o[t][2], o[t][3]};
            *(LAS f32x2*)(L + L_ML + (qd * 4 + wv4) * 8) = f32x2{m, lsum};
            STAMP(23 + layer);
        }
        __syncthreads();
        if (layer == 1 || *(const LAS unsigned*)(L + L_TL + 128) != 0u) break;
        __syncthreads();
        }
        {
            const int h = wave >> 1, dim = tid & 127;
            const f32x2 ml = *(const LAS f32x2*)(L + L_ML + (h * 4 + (lane & 3)) * 8);
            float ov[4];
#pragma unroll
            for (int w = 0; w < 4; ++w) ov[w] = *(const LAS float*)(L + L_OL + w * 2048 + (h >> 1) * 1024 + dim * 8 + (h & 1) * 4);
            float M = ml.x;
            MAX_DPP(M, "row_ror:2"); MAX_DPP(M, "row_ror:1");
            const float f = __builtin_amdgcn_exp2f(ml.x - M);
            float o = 0.f;
#pragma unroll
            for (int w = 0; w < 4; ++w) o = __builtin_fmaf(__builtin_bit_cast(float, __builtin_amdgcn_readlane(__builtin_bit_cast(int, f), w)), ov[w], o);
            const unsigned slot = (unsigned)((g * 2 + (h >> 1)) * 32 + i);
            __builtin_amdgcn_raw_buffer_store_b32(canon(o), rs, WS_OP + layer * OP_L + slot * 1024 + (tid & 255) * 4, 0, AUX_SC1);
            float Ls = f * ml.y;
            Ls += dpp<ROR2>(Ls); Ls += dpp<ROR1>(Ls);
            if (dim == 0) __builtin_amdgcn_raw_buffer_store_b64(v2u{canon(M), canon(Ls)}, rs, WS_ML + layer * ML_L + slot * 16 + (h & 1) * 8, 0, AUX_SC1);
        }
        STAMP(5 + 8 * layer);
        if (layer == 0) __builtin_amdgcn_s_sleep(D_E2A); else __builtin_amdgcn_s_sleep(D_E2);
        {
            const int sp16 = lane >> 2, q4 = lane & 3;
            const unsigned ob = WS_OP + layer * OP_L + (team * 32 + sp16) * 1024 + (32 * wave + 4 * q4) * 4;
            const unsigned mb = WS_ML + layer * ML_L + (team * 32 + sp16) * 16 + (wave >> 2) * 8;
            v4u oa0, oa1, ob0, ob1; v2u mla, mlb;
#pragma unroll 1
            for (unsigned sp = 0; sp < SPIN_CAP; ++sp) {
                FENCE();
                oa0 = __builtin_amdgcn_raw_buffer_load_b128(rs, ob, 0, AUX_SC1);
                oa1 = __builtin_amdgcn_raw_buffer_load_b128(rs, ob + 64, 0, AUX_SC1);
                ob0 = __builtin_amdgcn_raw_buffer_load_b128(rs, ob + 16384, 0, AUX_SC1);
                ob1 = __builtin_amdgcn_raw_buffer_load_b128(rs, ob + 16384 + 64, 0, AUX_SC1);
                mla = __builtin_amdgcn_raw_buffer_load_b64(rs, mb, 0, AUX_SC1);
                mlb = __builtin_amdgcn_raw_buffer_load_b64(rs, mb + 256, 0, AUX_SC1);
                const unsigned mx = max(max(umax3(umax4(oa0), umax4(oa1), mla.x), umax3(umax4(ob0), umax4(ob1), mla.y)), max(mlb.x, mlb.y));
                if (__all(mx != SENT)) break;
                __builtin_amdgcn_s_sleep(2);
            }
            STAMP(6 + 8 * layer);
            const float ma = __uint_as_float(mla.x), mbv = __uint_as_float(mlb.x);
            float M;
            asm("v_max_f32 %0, %1, %2" : "=v"(M) : "v"(ma), "v"(mbv));
            MAX_DPP(M, "row_ror:8"); MAX_DPP(M, "row_ror:4");
            M = swap32_max(swap16_max(M));
            const float fa = __builtin_amdgcn_exp2f(ma - M), fb = __builtin_amdgcn_exp2f(mbv - M);
            float Ls = __builtin_fmaf(fa, __uint_as_float(mla.y), fb * __uint_as_float(mlb.y));
            Ls += dpp<ROR8>(Ls); Ls += dpp<ROR4>(Ls);
            Ls = swap16_sum(Ls, Ls); Ls = swap32_sum(Ls, Ls);
            const f32x4 a = __builtin_bit_cast(f32x4, oa0) * fa + __builtin_bit_cast(f32x4, ob0) * fb;
            const f32x4 c = __builtin_bit_cast(f32x4, oa1) * fa + __builtin_bit_cast(f32x4, ob1) * fb;
            float v[4];
#pragma unroll
            for (int k = 0; k < 4; ++k) v[k] = swap32_sum(a[k], c[k]);
            float w0 = swap16_sum(v[0], v[2]), w1v = swap16_sum(v[1], v[3]);
            w0 += dpp<ROR8>(w0); w1v += dpp<ROR8>(w1v);
            w0 += dpp<ROR4>(w0); w1v += dpp<ROR4>(w1v);
            const float rl = __builtin_amdgcn_rcpf(Ls);
            if ((lane & 12) == 0) *(LAS unsigned*)(L + L_OB + (32 * wave + 16 * (qd >> 1) + 4 * q4 + 2 * (qd & 1)) * 2) = cvtpk(w0 * rl, w1v * rl);
        }
        if (layer == 0) {
            *(LAS v4u*)(L + L_WO + (32 * wave + (lane >> 2)) * 80 + 16 * (lane & 3)) = st0;
            *(LAS v4u*)(L + L_WO + (32 * wave + 16 + (lane >> 2)) * 80 + 16 * (lane & 3)) = st1;
        }
        __syncthreads();
        if (wave < 2) {
            if (layer == 0) {
#pragma unroll
                for (int ks = 0; ks < 8; ++ks) {
                    const LAS unsigned char* p = L + L_WO + (32 * ks + 8 * qd + (c16 >> 2)) * 80 + (16 * wave + 4 * (c16 & 3)) * 2;
                    const v2u lo = __builtin_bit_cast(v2u, __builtin_amdgcn_ds_read_tr16_b64_v4i16((LAS v4s*)p));
                    const v2u hi = __builtin_bit_cast(v2u, __builtin_amdgcn_ds_read_tr16_b64_v4i16((LAS v4s*)(p + 4 * 80)));
                    fr[4 + ks] = v4u{lo.x, lo.y, hi.x, hi.y};
                }
            }
            v4u of[8];
#pragma unroll
            for (int ks = 0; ks < 8; ++ks) of[ks] = *(const LAS v4u*)(L + L_OB + 64 * ks + 16 * qd);
            __builtin_amdgcn_sched_barrier(0);
            f32x4 d = {0.f, 0.f, 0.f, 0.f}, e = {0.f, 0.f, 0.f, 0.f};
#pragma unroll
            for (int ks = 0; ks < 8; ks += 2) { d = mfma(of[ks], fr[4 + ks], d); e = mfma(of[ks + 1], fr[5 + ks], e); }
            d[0] += e[0];
            if (lane < 16) __builtin_amdgcn_raw_buffer_store_b32(canon(d[0]), rs, WS_XP + layer * XP_L + (team * 512 + 32 * j + 16 * wave + lane) * 4, 0, AUX_SC1);
        }
        STAMP(7 + 8 * layer);
        if (layer == 1 && b != 0) break;
        if (layer == 0) {
            __builtin_amdgcn_s_sleep(D_E3A);
            v4u tv;
#pragma unroll 1
            for (unsigned sp = 0; sp < SPIN_CAP; ++sp) {
                FENCE();
                tv = __builtin_amdgcn_raw_buffer_load_b128(rs, WS_TAB + tid * 16, 0, AUX_SC1);
                const unsigned mx = max(umax4(tv), umax4(tv << 16));
                if (__all(mx < 0xFFFF0000u)) break;
                __builtin_amdgcn_s_sleep(2);
            }
            __syncthreads();
            *(LAS v4u*)(L + (tid < 256 ? L_TABP : L_TABN - 4096) + tid * 16) = tv;
        } else {
            __builtin_amdgcn_s_sleep(D_E3);
        }
        {
            const unsigned xb = WS_XP + layer * XP_L + (4 * qd * 512 + 64 * wave + 4 * c16) * 4;
            v4u xv[4];
#pragma unroll 1
            for (unsigned sp = 0; sp < SPIN_CAP; ++sp) {
                FENCE();
#pragma unroll
                for (int k = 0; k < 4; ++k) xv[k] = __builtin_amdgcn_raw_buffer_load_b128(rs, xb + k * 2048, 0, AUX_SC1);
                const unsigned mx = max(max(umax4(xv[0]), umax4(xv[1])), max(umax4(xv[2]), umax4(xv[3])));
                if (__all(mx != SENT)) break;
                __builtin_amdgcn_s_sleep(2);
            }
            const f32x4 v = (__builtin_bit_cast(f32x4, xv[0]) + __builtin_bit_cast(f32x4, xv[1])) + (__builtin_bit_cast(f32x4, xv[2]) + __builtin_bit_cast(f32x4, xv[3]));
            const float a0 = swap32_sum(v[0], v[2]), a1 = swap32_sum(v[1], v[3]);
            const float a = swap16_sum(a0, a1);
            *(LAS float*)(L + L_AL + (64 * wave + 4 * c16 + qd) * 4) = rbf(a);
        }
        __syncthreads();
        STAMP(8 + 8 * layer);
        if (wave < 4) {
            const f32x4 a0 = *(const LAS f32x4*)(L + L_AL + 32 * lg), a1 = *(const LAS f32x4*)(L + L_AL + 32 * lg + 16);
            { f32x2 t = unpk(xp.x) + f32x2{a0[0], a0[1]}; xp.x = cvtpk(t.x, t.y); }
            { f32x2 t = unpk(xp.y) + f32x2{a0[2], a0[3]}; xp.y = cvtpk(t.x, t.y); }
            { f32x2 t = unpk(xp.z) + f32x2{a1[0], a1[1]}; xp.z = cvtpk(t.x, t.y); }
            { f32x2 t = unpk(xp.w) + f32x2{a1[2], a1[3]}; xp.w = cvtpk(t.x, t.y); }
            const v4u h2 = layernorm_pk(layernorm_pk(xp));
            unsigned yv[8], big = 0;
#pragma unroll
            for (int k = 0; k < 4; ++k) {
                yv[2 * k] = *(const LAS unsigned short*)(L + ((h2[k] & 0xFFFFu) << 1));
                yv[2 * k + 1] = *(const LAS unsigned short*)(L + ((h2[k] >> 16) << 1));
                big |= ((h2[k] & 0x7FFF7FFFu) + 0x47004700u) ^ 0x80008000u;
            }
            big &= 0xF800F800u;
            if (__any(big != 0u)) {
#pragma unroll 1
                for (int k = 0; k < 8; ++k) {
                    unsigned hw = h2[0];
#pragma unroll
                    for (int kk = 1; kk < 4; ++kk) hw = ((k >> 1) == kk) ? h2[kk] : hw;
                    const unsigned u = (k & 1) ? (hw >> 16) : (hw & 0xFFFFu);
                    unsigned long long mk = __ballot(((u & 0x7FFFu) - (unsigned)(TAB_E0 << 7)) >= 2048u);
#pragma unroll 1
                    while (mk) {
                        const int ln = __builtin_ctzll(mk);
                        mk &= mk - 1;
                        const float tv = __uint_as_float((unsigned)__builtin_amdgcn_readlane((int)u, ln) << 16);
                        float a = 0.f;
#pragma unroll 1
                        for (int p = lane; p < FF / 2; p += 64) {
                            const unsigned ww1 = *(const unsigned*)(w1 + 2 * p), bb1 = *(const unsigned*)(b1 + 2 * p), ww2 = *(const unsigned*)(w2 + 2 * p);
                            a = mlp_pair(tv, unpk(ww1), unpk(bb1), ww2, a);
                        }
                        const unsigned y = cvtpk(rbf(wave_sum(a)) + b2f, 0.f) & 0xFFFFu;
#pragma unroll
                        for (int kk = 0; kk < 8; ++kk) yv[kk] = (k == kk && lane == ln) ? y : yv[kk];
                    }
                }
            }
#pragma unroll
            for (int k = 0; k < 4; ++k) {
                const f32x2 t = unpk(xp[k]) + f32x2{__uint_as_float(yv[2 * k] << 16), __uint_as_float(yv[2 * k + 1] << 16)};
                xp[k] = cvtpk(t.x, t.y);
            }
            if (layer == 0) {
                stage_q(layernorm_pk(xp), 1);
                STAMP(9);
            } else if (wave == 0) {
                *(v4u*)(out + 8 * lg) = xp;
            }
        }
    }
    STAMP(17);
    STAMP_DUMP();
}

extern "C" void kernel_launch(void* const* d_in, const int* in_sizes, int n_in, void* d_out, int out_size, void* d_ws,
                              size_t ws_size, hipStream_t stream) {
    const bf16_t* x = (const bf16_t*)d_in[0];
    const bf16_t* kc = (const bf16_t*)d_in[1];
    const bf16_t* vc = (const bf16_t*)d_in[2];
    const bf16_t* wq = (const bf16_t*)d_in[3];
    const bf16_t* wo = (const bf16_t*)d_in[4];
    const bf16_t* w1 = (const bf16_t*)d_in[6];
    const bf16_t* b1 = (const bf16_t*)d_in[7];
    const bf16_t* w2 = (const bf16_t*)d_in[8];
    const bf16_t* b2 = (const bf16_t*)d_in[9];
    (void)hipFuncSetAttribute((const void*)fused_kernel, hipFuncAttributeMaxDynamicSharedMemorySize, LDS_BYTES);
    fused_kernel<<<NWG, NTHR, LDS_BYTES, stream>>>(x, kc, vc, wq, wo, w1, b1, w2, b2, (bf16_t*)d_out, d_ws);
}
```

```cpp
#include <hip/hip_runtime.h>
#include <hip/hip_bf16.h>
#include <stdint.h>

typedef unsigned short bf16_t;
typedef unsigned v4u __attribute__((ext_vector_type(4)));
typedef unsigned v2u __attribute__((ext_vector_type(2)));
typedef float f32x4 __attribute__((ext_vector_type(4)));
typedef float f32x2 __attribute__((ext_vector_type(2)));
typedef short bf16x8 __attribute__((ext_vector_type(8)));
typedef short v4s __attribute__((ext_vector_type(4)));
typedef __bf16 bf16x2 __attribute__((ext_vector_type(2)));
#define LAS __attribute__((address_space(3)))

#define D_E1 10
#define D_E2 12
#define D_E2A 4
#define D_E3L 8
#define D_E3 18
#define D_K0 12
#define D_FL 70
#define D_FR 8
#define KB1 31
#define S1_STEP 2
#define CODE_PF 28672

constexpr int DM = 512, SK = 65536, WIN = 4096, FF = 1792;
constexpr int NWG = 256, NTHR = 640;
constexpr unsigned SENT = 0xFFFFFFFFu;
constexpr int AUX_SC1 = 16, AUX_NT = 2;
constexpr unsigned SPIN_CAP = 1u << 20;

constexpr unsigned WS_EPOCH = 0;
constexpr unsigned WS_FLAGS = 4096;
constexpr unsigned WS_TAB = 8192;
constexpr unsigned WS_QP = 16384;
constexpr unsigned QP_L = 8 * 4 * 512 * 4;
constexpr unsigned WS_OP = WS_QP + 2 * QP_L;
constexpr unsigned OP_L = 8 * 2 * 32 * 256 * 4;
constexpr unsigned WS_ML = WS_OP + 2 * OP_L;
constexpr unsigned ML_L = 8 * 2 * 32 * 4 * 4;
constexpr unsigned WS_XP = WS_ML + 2 * ML_L;
constexpr unsigned XP_L = 16 * 512 * 4;
constexpr unsigned WS_END = WS_XP + 2 * XP_L;

constexpr int L_K = 0;
constexpr int L_V = 32768;
constexpr int L_WQ = 65536;
constexpr int L_WO = L_WQ;
constexpr int L_OL = 102400;
constexpr int L_PL = L_OL + 8192;
constexpr int L_AL = L_PL + 2048;
constexpr int L_OB = L_AL + 2048;
constexpr int L_ML = L_OB + 512;
constexpr int L_QS = L_ML + 128;
constexpr int L_TL = L_QS + 1280;
constexpr int L_MW = L_TL + 128 + 16;
constexpr int L_END = L_MW + 3 * 3584;
constexpr int LDS_BYTES = 144 * 1024;
static_assert(L_END <= LDS_BYTES, "lds");
static_assert(L_QS % 16 == 0 && L_TL % 16 == 0 && L_OL % 16 == 0, "align");
constexpr int L_TABP = 2 * (114 << 7), L_TABN = 2 * (0x8000 + (114 << 7));
static_assert(L_TABP + 4096 <= L_WQ && L_TABN >= L_WO + 256 * 80 && L_TABN + 4096 <= L_OL && 131072 <= LDS_BYTES, "table placement");

constexpr int TAB_E0 = 114;

#define FENCE() asm volatile("" ::: "memory")

__device__ __forceinline__ float bflo(unsigned p) { return __uint_as_float(p << 16); }
__device__ __forceinline__ float bfhi(unsigned p) { return __uint_as_float(p & 0xFFFF0000u); }
__device__ __forceinline__ unsigned cvtpk(float a, float b) {
    f32x2 v = {a, b};
    return __builtin_bit_cast(unsigned, __builtin_convertvector(v, bf16x2));
}
__device__ __forceinline__ float rbf(float a) { return bflo(cvtpk(a, a)); }
__device__ __forceinline__ float dot2(unsigned a, unsigned b, float c) {
    return __builtin_amdgcn_fdot2_f32_bf16(__builtin_bit_cast(bf16x2, a), __builtin_bit_cast(bf16x2, b), c, false);
}
template <int CTRL> __device__ __forceinline__ float dpp(float x) {
    return __builtin_bit_cast(float, __builtin_amdgcn_mov_dpp(__builtin_bit_cast(int, x), CTRL, 0xf, 0xf, true));
}
constexpr int ROR1 = 0x121, ROR2 = 0x122, ROR4 = 0x124, ROR8 = 0x128;
__device__ __forceinline__ float swap32_sum(float a, float b) {
    auto r = __builtin_amdgcn_permlane32_swap(__float_as_uint(a), __float_as_uint(b), false, false);
    return __uint_as_float(r[0]) + __uint_as_float(r[1]);
}
__device__ __forceinline__ float swap16_sum(float a, float b) {
    auto r = __builtin_amdgcn_permlane16_swap(__float_as_uint(a), __float_as_uint(b), false, false);
    return __uint_as_float(r[0]) + __uint_as_float(r[1]);
}
__device__ __forceinline__ float swap32_max(float a) {
    auto r = __builtin_amdgcn_permlane32_swap(__float_as_uint(a), __float_as_uint(a), false, false);
    float o;
    asm("v_max_f32 %0, %1, %2" : "=v"(o) : "v"(r[0]), "v"(r[1]));
    return o;
}
__device__ __forceinline__ float swap16_max(float a) {
    auto r = __builtin_amdgcn_permlane16_swap(__float_as_uint(a), __float_as_uint(a), false, false);
    float o;
    asm("v_max_f32 %0, %1, %2" : "=v"(o) : "v"(r[0]), "v"(r[1]));
    return o;
}
__device__ __forceinline__ float row16_sum(float v) {
    v += dpp<ROR8>(v); v += dpp<ROR4>(v); v += dpp<ROR2>(v); v += dpp<ROR1>(v);
    return v;
}
#define MAX_DPP(v, CTRL) asm volatile("s_nop 1\n\tv_max_f32_dpp %0, %0, %0 " CTRL " row_mask:0xf bank_mask:0xf" : "+v"(v))
__device__ __forceinline__ float row16_max(float v) {
    MAX_DPP(v, "row_ror:8"); MAX_DPP(v, "row_ror:4"); MAX_DPP(v, "row_ror:2"); MAX_DPP(v, "row_ror:1");
    return v;
}
__device__ __forceinline__ float row8_max(float v) {
    MAX_DPP(v, "row_ror:4"); MAX_DPP(v, "row_ror:2"); MAX_DPP(v, "row_ror:1");
    return v;
}
__device__ __forceinline__ float wave_sum(float v) { v = row16_sum(v); v = swap16_sum(v, v); return swap32_sum(v, v); }
__device__ __forceinline__ unsigned canon(float v) {
    unsigned u = __float_as_uint(v);
    return u < SENT ? u : 0x7FC00000u;
}
__device__ __forceinline__ unsigned umax3(unsigned a, unsigned b, unsigned c) { return max(max(a, b), c); }
__device__ __forceinline__ unsigned umax4(v4u a) { return max(umax3(a.x, a.y, a.z), a.w); }
__device__ __forceinline__ f32x4 mfma(v4u a, v4u b, f32x4 c) {
    return __builtin_amdgcn_mfma_f32_16x16x32_bf16(__builtin_bit_cast(bf16x8, a), __builtin_bit_cast(bf16x8, b), c, 0, 0, 0);
}
__device__ __forceinline__ float mlp_pair(float t, f32x2 w1, f32x2 b1, unsigned w2p, float acc) {
    const f32x2 c1 = {-2.3022081986f, -2.3022081986f}, c2 = {-0.1029432396f, -0.1029432396f};
    const f32x2 tt = {t, t};
    const f32x2 a = tt * w1;
    const unsigned p = cvtpk(a.x, a.y);
    f32x2 u = {bflo(p), bfhi(p)};
    u += b1;
    const unsigned q = cvtpk(u.x, u.y);
    u.x = bflo(q); u.y = bfhi(q);
    const f32x2 z = u * __builtin_elementwise_fma(u * u, c2, c1);
    f32x2 e = {__builtin_amdgcn_exp2f(z.x), __builtin_amdgcn_exp2f(z.y)};
    e += 1.0f;
    const f32x2 r = {__builtin_amdgcn_rcpf(e.x), __builtin_amdgcn_rcpf(e.y)};
    const f32x2 gl = u * r;
    return dot2(cvtpk(gl.x, gl.y), w2p, acc);
}

__device__ __forceinline__ void mlp_pair2(float t0, float t1, f32x2 w1, f32x2 b1, unsigned w2p, float& acc0, float& acc1) {
    const f32x2 c1 = {-2.3022081986f, -2.3022081986f}, c2 = {-0.1029432396f, -0.1029432396f};
    const f32x2 a0 = f32x2{t0, t0} * w1, a1 = f32x2{t1, t1} * w1;
    const unsigned p0 = cvtpk(a0.x, a0.y), p1 = cvtpk(a1.x, a1.y);
    f32x2 u0 = {bflo(p0), bfhi(p0)}, u1 = {bflo(p1), bfhi(p1)};
    u0 += b1; u1 += b1;
    const unsigned q0 = cvtpk(u0.x, u0.y), q1 = cvtpk(u1.x, u1.y);
    u0 = f32x2{bflo(q0), bfhi(q0)}; u1 = f32x2{bflo(q1), bfhi(q1)};
    const f32x2 s0 = u0 * u0, s1 = u1 * u1;
    const f32x2 y0 = __builtin_elementwise_fma(s0, c2, c1), y1 = __builtin_elementwise_fma(s1, c2, c1);
    const f32x2 z0 = u0 * y0, z1 = u1 * y1;
    f32x2 e0 = {__builtin_amdgcn_exp2f(z0.x), __builtin_amdgcn_exp2f(z0.y)}, e1 = {__builtin_amdgcn_exp2f(z1.x), __builtin_amdgcn_exp2f(z1.y)};
    e0 += 1.0f; e1 += 1.0f;
    const f32x2 r0 = {__builtin_amdgcn_rcpf(e0.x), __builtin_amdgcn_rcpf(e0.y)}, r1 = {__builtin_amdgcn_rcpf(e1.x), __builtin_amdgcn_rcpf(e1.y)};
    const f32x2 g0 = u0 * r0, g1 = u1 * r1;
    const unsigned h0 = cvtpk(g0.x, g0.y), h1 = cvtpk(g1.x, g1.y);
    acc0 = dot2(h0, w2p, acc0); acc1 = dot2(h1, w2p, acc1);
}
__device__ __forceinline__ f32x2 unpk(unsigned p) { return f32x2{bflo(p), bfhi(p)}; }
__device__ __forceinline__ v4u layernorm_pk(v4u xp) {
    const unsigned ONES = 0x3F803F80u;
    float s1 = dot2(xp.x, ONES, 0.f), s2 = dot2(xp.x, xp.x, 0.f);
    s1 = dot2(xp.y, ONES, s1); s2 = dot2(xp.y, xp.y, s2);
    s1 = dot2(xp.z, ONES, s1); s2 = dot2(xp.z, xp.z, s2);
    s1 = dot2(xp.w, ONES, s1); s2 = dot2(xp.w, xp.w, s2);
    f32x2 d[4];
#pragma unroll
    for (int k = 0; k < 4; ++k) d[k] = unpk(xp[k]);
    s1 += dpp<ROR8>(s1); s2 += dpp<ROR8>(s2);
    s1 += dpp<ROR4>(s1); s2 += dpp<ROR4>(s2);
    s1 += dpp<ROR2>(s1); s2 += dpp<ROR2>(s2);
    s1 += dpp<ROR1>(s1); s2 += dpp<ROR1>(s2);
    float t = swap16_sum(s1, s2);
    t = swap32_sum(t, t);
    auto r2 = __builtin_amdgcn_permlane16_swap(__float_as_uint(t), __float_as_uint(t), false, false);
    const float mean = __uint_as_float(r2[0]) * (1.0f / DM);
    const float msq = mean * mean;
    float var = __builtin_fmaf(__uint_as_float(r2[1]), 1.0f / DM, -msq);
    const float m = rbf(mean);
    const f32x2 mm = {m, m};
#pragma unroll
    for (int k = 0; k < 4; ++k) d[k] -= mm;
    if (__builtin_expect(!(msq <= 64.f * var), 0)) {
        f32x2 q = d[0] * d[0];
#pragma unroll
        for (int k = 1; k < 4; ++k) q = __builtin_elementwise_fma(d[k], d[k], q);
        const float dm = mean - m;
        var = fmaxf(__builtin_fmaf(-dm, dm, wave_sum(q.x + q.y) * (1.0f / DM)), 0.f);
    }
    const float sd = rbf(__builtin_amdgcn_sqrtf(rbf(rbf(var) + 1e-5f)));
    const float r = __builtin_amdgcn_rcpf(sd);
    const f32x2 rr = {r, r};
    v4u o;
#pragma unroll
    for (int k = 0; k < 4; ++k) {
        const f32x2 tt = unpk(cvtpk(d[k].x, d[k].y)) * rr;
        o[k] = cvtpk(tt.x, tt.y);
    }
    return o;
}

extern "C" __global__ void __launch_bounds__(NTHR, 3)
fused_kernel(const bf16_t* x, const bf16_t* kc, const bf16_t* vc, const bf16_t* wq, const bf16_t* wo, const bf16_t* w1,
             const bf16_t* b1, const bf16_t* w2, const bf16_t* b2, bf16_t* out, void* ws) {
    extern __shared__ __attribute__((aligned(16))) unsigned char smem[];
    LAS unsigned char* L = (LAS unsigned char*)smem;
    const int b = blockIdx.x, tid = threadIdx.x, lane = tid & 63;
    const int wave = __builtin_amdgcn_readfirstlane(tid >> 6);
    const int g = b & 7, i = b >> 3, hp = i & 1, j = i >> 1, cb = i & 7, rb = i >> 3;
    const __amdgpu_buffer_rsrc_t rs = __builtin_amdgcn_make_buffer_rsrc(ws, 0, (int)WS_END, 0x00020000);
    const unsigned team = (unsigned)(g * 2 + hp);

    auto gather_q = [&](int layer) {
        const unsigned base = WS_QP + layer * QP_L + g * 8192 + lane * 16;
        v4u qv[8];
#pragma unroll 1
        for (unsigned sp = 0; sp < SPIN_CAP; ++sp) {
            FENCE();
#pragma unroll
            for (int k = 0; k < 8; ++k) qv[k] = __builtin_amdgcn_raw_buffer_load_b128(rs, base + k * 1024, 0, AUX_SC1);
            unsigned mx = 0;
#pragma unroll
            for (int k = 0; k < 8; ++k) mx = max(mx, umax4(qv[k]));
            if (__all(mx != SENT)) break;
            __builtin_amdgcn_s_sleep(2);
        }
#pragma unroll
        for (int hf = 0; hf < 2; ++hf) {
            const f32x4 q = (__builtin_bit_cast(f32x4, qv[hf]) + __builtin_bit_cast(f32x4, qv[2 + hf])) + (__builtin_bit_cast(f32x4, qv[4 + hf]) + __builtin_bit_cast(f32x4, qv[6 + hf]));
            *(LAS v2u*)(L + L_QS + hf * 512 + lane * 8) = v2u{cvtpk(q[0], q[1]), cvtpk(q[2], q[3])};
        }
    };

    if (wave == 9) {
        __builtin_amdgcn_s_setprio(3);
        if (i < 4) {
            unsigned plo, phi, csz, csz2;
            asm volatile("s_getpc_b64 s[90:91]\n"
                         ".Lpf_here%=:\n\t"
                         "s_sub_u32 %0, s90, .Lpf_here%=-fused_kernel\n\t"
                         "s_subb_u32 %1, s91, 0\n\t"
                         "s_mov_b32 %2, .Lpf_mark_a-fused_kernel\n\t"
                         "s_mov_b32 %3, .Lpf_mark_b-fused_kernel"
                         : "=s"(plo), "=s"(phi), "=s"(csz), "=s"(csz2) :: "s90", "s91", "scc");
            csz = max(csz, csz2);
            const __amdgpu_buffer_rsrc_t rc = __builtin_amdgcn_make_buffer_rsrc((void*)(((unsigned long long)phi << 32) | plo), 0, (int)(csz & ~15u), 0x00020000);
#pragma unroll
            for (int k = 0; k < CODE_PF / 4096; ++k)
                __builtin_amdgcn_raw_ptr_buffer_load_lds(rc, (LAS unsigned*)(L + L_OL), 16, min((unsigned)(i * (CODE_PF / 4) + k * 1024 + lane * 16), (csz & ~15u) - 16u), 0, 0, 0);
        }
        const size_t kvb = ((size_t)g * SK + (SK - WIN) + 128 * i) * 128;
        const __amdgpu_buffer_rsrc_t rk = __builtin_amdgcn_make_buffer_rsrc((void*)(kc + kvb), 0, 32768, 0x00020000);
        const __amdgpu_buffer_rsrc_t rv = __builtin_amdgcn_make_buffer_rsrc((void*)(vc + kvb), 0, 32768, 0x00020000);
        int ko[4], vo[4];
#pragma unroll
        for (int k = 0; k < 4; ++k) {
            const int row = 4 * k + (lane >> 4);
            ko[k] = row * 256 + 16 * ((lane & 15) ^ row);
            vo[k] = row * 256 + 16 * ((lane & 15) ^ (2 * ((row & 3) | ((row >> 3) << 2))));
        }
        __builtin_amdgcn_s_barrier();
        __builtin_amdgcn_s_sleep(D_K0);
#pragma unroll
        for (int it = 0; it < 64; ++it) {
            if (it < 32) __builtin_amdgcn_raw_ptr_buffer_load_lds(rk, (LAS unsigned*)(L + L_K + it * 1024), 16, ko[it & 3], (it >> 2) * 4096, 0, AUX_NT);
            else __builtin_amdgcn_raw_ptr_buffer_load_lds(rv, (LAS unsigned*)(L + L_V + (it - 32) * 1024), 16, vo[it & 3], ((it - 32) >> 2) * 4096, 0, AUX_NT);
            if (it == KB1) __builtin_amdgcn_s_barrier();
        }
        asm volatile("s_waitcnt vmcnt(0)" ::: "memory");
        __builtin_amdgcn_s_barrier();
        return;
    }
    if (wave == 8) {
        __builtin_amdgcn_s_setprio(3);
        const v2u ep = __builtin_amdgcn_raw_buffer_load_b64(rs, WS_EPOCH, 0, AUX_SC1);
        const v4u sv = {SENT, SENT, SENT, SENT};
#pragma unroll
        for (int l2 = 0; l2 < 2; ++l2) {
            __builtin_amdgcn_raw_buffer_store_b128(sv, rs, WS_OP + l2 * OP_L + ((g * 2) * 32 + i) * 1024 + lane * 16, 0, AUX_SC1);
            __builtin_amdgcn_raw_buffer_store_b128(sv, rs, WS_OP + l2 * OP_L + ((g * 2 + 1) * 32 + i) * 1024 + lane * 16, 0, AUX_SC1);
            unsigned off;
            if (lane < 16) off = WS_QP + l2 * QP_L + ((g * 4 + rb) * 512 + cb * 64) * 4 + lane * 16;
            else if (lane < 24) off = WS_XP + l2 * XP_L + (team * 512 + j * 32) * 4 + (lane - 16) * 16;
            else if (lane < 26) off = WS_ML + l2 * ML_L + ((g * 2 + (lane - 24)) * 32 + i) * 16;
            else off = WS_TAB + b * 32 + (lane & 1) * 16;
            if (lane < 28) __builtin_amdgcn_raw_buffer_store_b128(sv, rs, off, 0, AUX_SC1);
        }
        *(LAS unsigned*)(L + L_QS + 1024 + lane * 4) = 0u;
        FENCE();
        __builtin_amdgcn_s_barrier();
        FENCE();
        const unsigned long long T = (((unsigned long long)ep.y << 32) | ep.x) + 0x9E3779B97F4A7C15ull;
        const unsigned tlo = __builtin_amdgcn_readfirstlane((unsigned)T), thi = __builtin_amdgcn_readfirstlane((unsigned)(T >> 32));
        asm volatile("s_waitcnt vmcnt(0)" ::: "memory");
        if (lane == 0) __builtin_amdgcn_raw_buffer_store_b64(v2u{tlo, thi}, rs, WS_FLAGS + b * 8, 0, AUX_SC1);
        __syncthreads();
        __builtin_amdgcn_s_sleep(D_FL);
        const unsigned qbase = WS_QP + g * 8192 + lane * 16;
        v4u qv[8];
#pragma unroll 1
        for (unsigned sp = 0; sp < SPIN_CAP; ++sp) {
            FENCE();
            const v4u f0 = __builtin_amdgcn_raw_buffer_load_b128(rs, WS_FLAGS + lane * 32, 0, AUX_SC1);
            const v4u f1 = __builtin_amdgcn_raw_buffer_load_b128(rs, WS_FLAGS + lane * 32 + 16, 0, AUX_SC1);
#pragma unroll
            for (int k = 0; k < 8; ++k) qv[k] = __builtin_amdgcn_raw_buffer_load_b128(rs, qbase + k * 1024, 0, AUX_SC1);
            const unsigned bad = (f0.x ^ tlo) | (f0.y ^ thi) | (f0.z ^ tlo) | (f0.w ^ thi) | (f1.x ^ tlo) | (f1.y ^ thi) | (f1.z ^ tlo) | (f1.w ^ thi);
            unsigned mx = 0;
#pragma unroll
            for (int k = 0; k < 8; ++k) mx = max(mx, umax4(qv[k]));
            if (__all(bad == 0u && mx != SENT)) break;
            __builtin_amdgcn_s_sleep(D_FR);
        }
        if (b == 0 && lane == 0) __builtin_amdgcn_raw_buffer_store_b64(v2u{tlo, thi}, rs, WS_EPOCH, 0, AUX_SC1);
#pragma unroll 1
        for (unsigned sp = 0; sp < SPIN_CAP; ++sp) {
            FENCE();
            v4u rv2[8];
#pragma unroll
            for (int k = 0; k < 8; ++k) rv2[k] = __builtin_amdgcn_raw_buffer_load_b128(rs, qbase + k * 1024, 0, AUX_SC1);
            FENCE();
#pragma unroll
            for (int hf = 0; hf < 2; ++hf) {
                const f32x4 q = (__builtin_bit_cast(f32x4, qv[hf]) + __builtin_bit_cast(f32x4, qv[2 + hf])) + (__builtin_bit_cast(f32x4, qv[4 + hf]) + __builtin_bit_cast(f32x4, qv[6 + hf]));
                *(LAS v2u*)(L + L_QS + hf * 512 + lane * 8) = v2u{cvtpk(q[0], q[1]), cvtpk(q[2], q[3])};
            }
            asm volatile("s_waitcnt lgkmcnt(0)" ::: "memory");
            __builtin_amdgcn_s_barrier();
            FENCE();
            unsigned diff = 0;
#pragma unroll
            for (int k = 0; k < 8; ++k) { const v4u d = rv2[k] ^ qv[k]; diff |= d.x | d.y | d.z | d.w; qv[k] = rv2[k]; }
            const bool same = __all(diff == 0u);
            if (!same) {
#pragma unroll 1
                for (unsigned s2 = 0; s2 < SPIN_CAP; ++s2) {
                    unsigned mx = 0;
#pragma unroll
                    for (int k = 0; k < 8; ++k) mx = max(mx, umax4(qv[k]));
                    if (__all(mx != SENT)) break;
                    __builtin_amdgcn_s_sleep(4);
                    FENCE();
#pragma unroll
                    for (int k = 0; k < 8; ++k) qv[k] = __builtin_amdgcn_raw_buffer_load_b128(rs, qbase + k * 1024, 0, AUX_SC1);
                }
            }
            if (lane == 0) *(LAS unsigned*)(L + L_TL + 128) = same ? 1u : 0u;
            __syncthreads();
            if (same) break;
        }
        asm volatile(".Lpf_mark_b:");
        return;
    }

    const int qd = lane >> 4, c16 = lane & 15, wv4 = wave & 3;
    const int lg = 16 * (c16 >> 2) + 4 * (c16 & 3) + qd;
    const int pg = tid & 127, eg = tid >> 7;
    unsigned w1p[7], b1p[7], w2p[7];
#pragma unroll
    for (int k = 0; k < 7; ++k) {
        w1p[k] = *(const unsigned*)(w1 + 2 * (pg + 128 * k));
        b1p[k] = *(const unsigned*)(b1 + 2 * (pg + 128 * k));
        w2p[k] = *(const unsigned*)(w2 + 2 * (pg + 128 * k));
    }
    const float b2f = __uint_as_float((unsigned)b2[0] << 16);
    const v4u xin = *(const v4u*)(x + 8 * lg);
    FENCE();
    const __amdgpu_buffer_rsrc_t rwq = __builtin_amdgcn_make_buffer_rsrc((void*)(wq + (size_t)(128 * rb) * 4096 + 512 * g + 64 * cb), 0, 128 * 8192, 0x00020000);
    v4u st0, st1;
    st0 = __builtin_amdgcn_raw_buffer_load_b128(rwq, (16 * wave + (lane >> 3)) * 8192 + 16 * (lane & 7), 0, AUX_NT);
    st1 = __builtin_amdgcn_raw_buffer_load_b128(rwq, (16 * wave + (lane >> 3) + 8) * 8192 + 16 * (lane & 7), 0, AUX_NT);
    FENCE();
    __builtin_amdgcn_s_barrier();
    FENCE();
    v4u xp = xin;
    v4u fr[16];
    const unsigned qp_off = WS_QP + ((g * 4 + rb) * 512 + cb * 64 + 16 * wv4 + c16) * 4;
    auto stage_q = [&](v4u h, int layer) {
        const f32x4 z = {0.f, 0.f, 0.f, 0.f};
        const f32x4 d0 = mfma(h, fr[0], z), d1 = mfma(h, fr[1], z), d2 = mfma(h, fr[2], z), d3 = mfma(h, fr[3], z);
        const float q = (d0[0] + d1[1]) + (d2[2] + d3[3]);
        if (qd == rb) __builtin_amdgcn_raw_buffer_store_b32(canon(q), rs, qp_off + layer * QP_L, 0, AUX_SC1);
    };

    v4u hn0 = {0u, 0u, 0u, 0u};
    if (wave < 4) hn0 = layernorm_pk(xp);
    {
        f32x2 w1f[7], b1f[7];
#pragma unroll
        for (int k = 0; k < 7; ++k) { w1f[k] = unpk(w1p[k]); b1f[k] = unpk(b1p[k]); }
        if (wave >= 6) {
#pragma unroll
            for (int k = 0; k < 7; ++k) {
                *(LAS unsigned*)(L + L_MW + 4 * (pg + 128 * k)) = w1p[k];
                *(LAS unsigned*)(L + L_MW + 3584 + 4 * (pg + 128 * k)) = b1p[k];
                *(LAS unsigned*)(L + L_MW + 7168 + 4 * (pg + 128 * k)) = w2p[k];
            }
        }
        float acc[4] = {0.f, 0.f, 0.f, 0.f};
#pragma unroll
        for (int u = 0; u < 4; u += 2) {
            const unsigned idx = (unsigned)(16 * b + 4 * eg + u);
            const float tv0 = __uint_as_float((((idx >> 11) << 15) | ((idx & 0x7FFu) + (TAB_E0 << 7))) << 16);
            const float tv1 = __uint_as_float(__float_as_uint(tv0) + 0x10000u);
#pragma unroll
            for (int k = 0; k < 7; ++k) {
                if (u == 0 && k == S1_STEP) {
                    asm volatile("" : "+v"(acc[0]), "+v"(acc[1]) :: "memory");
                    *(LAS v4u*)(L + L_WQ + (16 * wave + (lane >> 3)) * 128 + 16 * (lane & 7)) = st0;
                    *(LAS v4u*)(L + L_WQ + (16 * wave + (lane >> 3) + 8) * 128 + 16 * (lane & 7)) = st1;
                    __syncthreads();
                    if (wave < 4) {
                        __builtin_amdgcn_s_setprio(2);
#pragma unroll
                        for (int ks = 0; ks < 4; ++ks) {
                            const LAS unsigned char* p = L + L_WQ + (32 * ks + 8 * qd + (c16 >> 2)) * 128 + (16 * wv4 + 4 * (c16 & 3)) * 2;
                            const v2u lo = __builtin_bit_cast(v2u, __builtin_amdgcn_ds_read_tr16_b64_v4i16((LAS v4s*)p));
                            const v2u hi = __builtin_bit_cast(v2u, __builtin_amdgcn_ds_read_tr16_b64_v4i16((LAS v4s*)(p + 4 * 128)));
                            fr[ks] = v4u{lo.x, lo.y, hi.x, hi.y};
                        }
                        stage_q(hn0, 0);
                        __builtin_amdgcn_s_setprio(0);
                    }
                    FENCE();
                    __builtin_amdgcn_sched_barrier(0);
                }
                mlp_pair2(tv0, tv1, w1f[k], b1f[k], w2p[k], acc[u], acc[u + 1]);
                __builtin_amdgcn_sched_barrier(0);
            }
        }
        acc[0] = swap32_sum(acc[0], acc[2]); acc[1] = swap32_sum(acc[1], acc[3]);
        acc[0] = row16_sum(swap16_sum(acc[0], acc[1]));
        if (c16 == 0) *(LAS float*)(L + L_TL + (wave * 4 + qd) * 4) = acc[0];
    }
    __syncthreads();
    {
        const __amdgpu_buffer_rsrc_t rwo = __builtin_amdgcn_make_buffer_rsrc((void*)(wo + (size_t)(512 * g + 256 * hp) * 512 + 32 * j), 0, 256 * 1024, 0x00020000);
        st0 = __builtin_amdgcn_raw_buffer_load_b128(rwo, (32 * wave + (lane >> 2)) * 1024 + 16 * (lane & 3), 0, AUX_NT);
        st1 = __builtin_amdgcn_raw_buffer_load_b128(rwo, (32 * wave + 16 + (lane >> 2)) * 1024 + 16 * (lane & 3), 0, AUX_NT);
        FENCE();
    }
    if (wave >= 4) {
#pragma unroll
        for (int T = 0; T < 2; ++T)
#pragma unroll
            for (int ks = 0; ks < 4; ++ks)
                fr[4 * T + ks] = *(const LAS v4u*)(L + L_K + (32 * wv4 + 16 * T + c16) * 256 + 16 * ((4 * ks + qd) ^ c16));
    } else if (tid < 16) {
        const int e2 = tid >> 2, u = tid & 3;
        const float sum = *(const LAS float*)(L + L_TL + ((2 * e2) * 4 + u) * 4) + *(const LAS float*)(L + L_TL + ((2 * e2 + 1) * 4 + u) * 4);
        unsigned y = cvtpk(rbf(sum) + b2f, 0.f) & 0xFFFFu;
        if (y == 0xFFFFu) y = 0x7FC0u;
        __builtin_amdgcn_raw_buffer_store_b16((unsigned short)y, rs, WS_TAB + (16 * b + tid) * 2, 0, AUX_SC1);
    }
    const int qa0 = L_QS + ((c16 & 3) == 0 ? (c16 >> 2) * 256 : 1024) + 16 * qd;
    const int qa1 = L_QS + ((c16 & 3) == 1 ? (c16 >> 2) * 256 : 1024) + 16 * qd;

#pragma unroll
    for (int layer = 0; layer < 2; ++layer) {
        if (layer == 1) {
            if (wave == 0) { __builtin_amdgcn_s_sleep(D_E1); gather_q(1); }
            __syncthreads();
        }
#pragma unroll 1
        for (unsigned redo = 0; redo < SPIN_CAP; ++redo) {
        if (wave >= 4) {
            v4u qf[8];
#pragma unroll
            for (int ks = 0; ks < 4; ++ks) { qf[2 * ks] = *(const LAS v4u*)(L + qa0 + 64 * ks); qf[2 * ks + 1] = *(const LAS v4u*)(L + qa1 + 64 * ks); }
            if (layer == 0 && redo == 0) {
                const int vrow = 32 * wv4 + 8 * qd + (c16 >> 2);
                const int vsw = 2 * ((vrow & 3) | (((vrow >> 3) & 1) << 2));
                const LAS unsigned char* vb = L + L_V + vrow * 256 + 8 * (c16 & 1);
#pragma unroll
                for (int t = 0; t < 8; ++t) {
                    const LAS unsigned char* p = vb + 16 * ((2 * t + ((c16 >> 1) & 1)) ^ vsw);
                    const v2u lo = __builtin_bit_cast(v2u, __builtin_amdgcn_ds_read_tr16_b64_v4i16((LAS v4s*)p));
                    const v2u hi = __builtin_bit_cast(v2u, __builtin_amdgcn_ds_read_tr16_b64_v4i16((LAS v4s*)(p + 4 * 256)));
                    fr[8 + t] = v4u{lo.x, lo.y, hi.x, hi.y};
                }
            }
            __builtin_amdgcn_sched_barrier(0);
            f32x4 d = {0.f, 0.f, 0.f, 0.f}, e = {0.f, 0.f, 0.f, 0.f};
#pragma unroll
            for (int ks = 0; ks < 4; ++ks) { d = mfma(qf[2 * ks], fr[ks], d); e = mfma(qf[2 * ks + 1], fr[4 + ks], e); }
            const unsigned sp = cvtpk(d[0], e[1]);
            const f32x2 s0 = unpk(sp) * 0.08838834764831845f;
            const f32x2 sc = unpk(cvtpk(s0.x, s0.y));
            float mx;
            asm("v_max_f32 %0, %1, %2" : "=v"(mx) : "v"(sc.x), "v"(sc.y));
            const float m = row16_max(mx) * 1.4426950408889634f;
            const float p0 = __builtin_amdgcn_exp2f(__builtin_fmaf(sc.x, 1.4426950408889634f, -m));
            const float p1 = __builtin_amdgcn_exp2f(__builtin_fmaf(sc.y, 1.4426950408889634f, -m));
            const unsigned ph = cvtpk(p0, p1);
            const unsigned pl = cvtpk(p0 - bflo(ph), p1 - bfhi(ph));
            LAS unsigned char* pw = L + L_PL + wv4 * 512 + (qd >> 1) * 256 + (qd & 1) * 64 + c16 * 2;
            *(LAS unsigned short*)pw = (unsigned short)ph;
            *(LAS unsigned short*)(pw + 32) = (unsigned short)(ph >> 16);
            *(LAS unsigned short*)(pw + 128) = (unsigned short)pl;
            *(LAS unsigned short*)(pw + 160) = (unsigned short)(pl >> 16);
            const v4u pf = *(const LAS v4u*)(L + L_PL + wv4 * 512 + (c16 & 7) * 64 + 16 * qd);
            const float lsum = row16_sum(p0 + p1);
            f32x4 o[8];
#pragma unroll
            for (int t = 0; t < 8; ++t) { const f32x4 z = {0.f, 0.f, 0.f, 0.f}; o[t] = mfma(pf, fr[8 + t], z); }
            __builtin_amdgcn_sched_barrier(0);
            LAS unsigned char* ow = L + L_OL + wv4 * 2048 + (qd & 1) * 1024 + c16 * 8;
#pragma unroll
            for (int t = 0; t < 8; ++t) *(LAS f32x2*)(ow + 128 * t) = f32x2{o[t][0], o[t][1]} + f32x2{o[t][2], o[t][3]};
            *(LAS f32x2*)(L + L_ML + (qd * 4 + wv4) * 8) = f32x2{m, lsum};
        }
        __syncthreads();
        if (layer == 1 || *(const LAS unsigned*)(L + L_TL + 128) != 0u) break;
        __syncthreads();
        }
        {
            const int h = wave >> 1, dim = tid & 127;
            const f32x2 ml = *(const LAS f32x2*)(L + L_ML + (h * 4 + (lane & 3)) * 8);
            float ov[4];
#pragma unroll
            for (int w = 0; w < 4; ++w) ov[w] = *(const LAS float*)(L + L_OL + w * 2048 + (h >> 1) * 1024 + dim * 8 + (h & 1) * 4);
            float M = ml.x;
            MAX_DPP(M, "row_ror:2"); MAX_DPP(M, "row_ror:1");
            const float f = __builtin_amdgcn_exp2f(ml.x - M);
            float o = 0.f;
#pragma unroll
            for (int w = 0; w < 4; ++w) o = __builtin_fmaf(__builtin_bit_cast(float, __builtin_amdgcn_readlane(__builtin_bit_cast(int, f), w)), ov[w], o);
            const unsigned slot = (unsigned)((g * 2 + (h >> 1)) * 32 + i);
            __builtin_amdgcn_raw_buffer_store_b32(canon(o), rs, WS_OP + layer * OP_L + slot * 1024 + (tid & 255) * 4, 0, AUX_SC1);
            float Ls = f * ml.y;
            Ls += dpp<ROR2>(Ls); Ls += dpp<ROR1>(Ls);
            if (dim == 0) __builtin_amdgcn_raw_buffer_store_b64(v2u{canon(M), canon(Ls)}, rs, WS_ML + layer * ML_L + slot * 16 + (h & 1) * 8, 0, AUX_SC1);
        }
        if (layer == 0) {
            __builtin_amdgcn_s_sleep(D_E2A);
            v4u tv;
#pragma unroll 1
            for (unsigned sp = 0; sp < SPIN_CAP; ++sp) {
                FENCE();
                tv = __builtin_amdgcn_raw_buffer_load_b128(rs, WS_TAB + tid * 16, 0, AUX_SC1);
                const unsigned mx = max(umax4(tv), umax4(tv << 16));
                if (__all(mx < 0xFFFF0000u)) break;
                __builtin_amdgcn_s_sleep(4);
            }
            *(LAS v4u*)(L + (tid < 256 ? L_TABP : L_TABN - 4096) + tid * 16) = tv;
        } else {
            __builtin_amdgcn_s_sleep(D_E2);
        }
        {
            const int sp16 = lane >> 2, q4 = lane & 3;
            const unsigned ob = WS_OP + layer * OP_L + (team * 32 + sp16) * 1024 + (32 * wave + 4 * q4) * 4;
            const unsigned mb = WS_ML + layer * ML_L + (team * 32 + sp16) * 16 + (wave >> 2) * 8;
            v4u oa0, oa1, ob0, ob1; v2u mla, mlb;
#pragma unroll 1
            for (unsigned sp = 0; sp < SPIN_CAP; ++sp) {
                FENCE();
                oa0 = __builtin_amdgcn_raw_buffer_load_b128(rs, ob, 0, AUX_SC1);
                oa1 = __builtin_amdgcn_raw_buffer_load_b128(rs, ob + 64, 0, AUX_SC1);
                ob0 = __builtin_amdgcn_raw_buffer_load_b128(rs, ob + 16384, 0, AUX_SC1);
                ob1 = __builtin_amdgcn_raw_buffer_load_b128(rs, ob + 16384 + 64, 0, AUX_SC1);
                mla = __builtin_amdgcn_raw_buffer_load_b64(rs, mb, 0, AUX_SC1);
                mlb = __builtin_amdgcn_raw_buffer_load_b64(rs, mb + 256, 0, AUX_SC1);
                const unsigned mx = max(max(umax3(umax4(oa0), umax4(oa1), mla.x), umax3(umax4(ob0), umax4(ob1), mla.y)), max(mlb.x, mlb.y));
                if (__all(mx != SENT)) break;
                __builtin_amdgcn_s_sleep(2);
            }
            const float ma = __uint_as_float(mla.x), mbv = __uint_as_float(mlb.x);
            float M;
            asm("v_max_f32 %0, %1, %2" : "=v"(M) : "v"(ma), "v"(mbv));
            MAX_DPP(M, "row_ror:8"); MAX_DPP(M, "row_ror:4");
            M = swap32_max(swap16_max(M));
            const float fa = __builtin_amdgcn_exp2f(ma - M), fb = __builtin_amdgcn_exp2f(mbv - M);
            float Ls = __builtin_fmaf(fa, __uint_as_float(mla.y), fb * __uint_as_float(mlb.y));
            Ls += dpp<ROR8>(Ls); Ls += dpp<ROR4>(Ls);
            Ls = swap16_sum(Ls, Ls); Ls = swap32_sum(Ls, Ls);
            const f32x4 a = __builtin_bit_cast(f32x4, oa0) * fa + __builtin_bit_cast(f32x4, ob0) * fb;
            const f32x4 c = __builtin_bit_cast(f32x4, oa1) * fa + __builtin_bit_cast(f32x4, ob1) * fb;
            float v[4];
#pragma unroll
            for (int k = 0; k < 4; ++k) v[k] = swap32_sum(a[k], c[k]);
            float w0 = swap16_sum(v[0], v[2]), w1v = swap16_sum(v[1], v[3]);
            w0 += dpp<ROR8>(w0); w1v += dpp<ROR8>(w1v);
            w0 += dpp<ROR4>(w0); w1v += dpp<ROR4>(w1v);
            const float rl = __builtin_amdgcn_rcpf(Ls);
            if ((lane & 12) == 0) *(LAS unsigned*)(L + L_OB + (32 * wave + 16 * (qd >> 1) + 4 * q4 + 2 * (qd & 1)) * 2) = cvtpk(w0 * rl, w1v * rl);
        }
        if (layer == 0) {
            *(LAS v4u*)(L + L_WO + (32 * wave + (lane >> 2)) * 80 + 16 * (lane & 3)) = st0;
            *(LAS v4u*)(L + L_WO + (32 * wave + 16 + (lane >> 2)) * 80 + 16 * (lane & 3)) = st1;
        }
        __syncthreads();
        if (wave < 2) {
            if (layer == 0) {
#pragma unroll
                for (int ks = 0; ks < 8; ++ks) {
                    const LAS unsigned char* p = L + L_WO + (32 * ks + 8 * qd + (c16 >> 2)) * 80 + (16 * wave + 4 * (c16 & 3)) * 2;
                    const v2u lo = __builtin_bit_cast(v2u, __builtin_amdgcn_ds_read_tr16_b64_v4i16((LAS v4s*)p));
                    const v2u hi = __builtin_bit_cast(v2u, __builtin_amdgcn_ds_read_tr16_b64_v4i16((LAS v4s*)(p + 4 * 80)));
                    fr[4 + ks] = v4u{lo.x, lo.y, hi.x, hi.y};
                }
            }
            v4u of[8];
#pragma unroll
            for (int ks = 0; ks < 8; ++ks) of[ks] = *(const LAS v4u*)(L + L_OB + 64 * ks + 16 * qd);
            __builtin_amdgcn_sched_barrier(0);
            f32x4 d = {0.f, 0.f, 0.f, 0.f}, e = {0.f, 0.f, 0.f, 0.f};
#pragma unroll
            for (int ks = 0; ks < 8; ks += 2) { d = mfma(of[ks], fr[4 + ks], d); e = mfma(of[ks + 1], fr[5 + ks], e); }
            d[0] += e[0];
            if (lane < 16) __builtin_amdgcn_raw_buffer_store_b32(canon(d[0]), rs, WS_XP + layer * XP_L + (team * 512 + 32 * j + 16 * wave + lane) * 4, 0, AUX_SC1);
        }
        if (layer == 1 && b != 0) break;
        if (layer == 0) __builtin_amdgcn_s_sleep(D_E3); else __builtin_amdgcn_s_sleep(D_E3L);
        {
            const unsigned xb = WS_XP + layer * XP_L + (4 * qd * 512 + 64 * wave + 4 * c16) * 4;
            v4u xv[4];
#pragma unroll 1
            for (unsigned sp = 0; sp < SPIN_CAP; ++sp) {
                FENCE();
#pragma unroll
                for (int k = 0; k < 4; ++k) xv[k] = __builtin_amdgcn_raw_buffer_load_b128(rs, xb + k * 2048, 0, AUX_SC1);
                const unsigned mx = max(max(umax4(xv[0]), umax4(xv[1])), max(umax4(xv[2]), umax4(xv[3])));
                if (__all(mx != SENT)) break;
                __builtin_amdgcn_s_sleep(2);
            }
            const f32x4 v = (__builtin_bit_cast(f32x4, xv[0]) + __builtin_bit_cast(f32x4, xv[1])) + (__builtin_bit_cast(f32x4, xv[2]) + __builtin_bit_cast(f32x4, xv[3]));
            const float a0 = swap32_sum(v[0], v[2]), a1 = swap32_sum(v[1], v[3]);
            const float a = swap16_sum(a0, a1);
            *(LAS float*)(L + L_AL + (64 * wave + 4 * c16 + qd) * 4) = rbf(a);
        }
        __syncthreads();
        if (wave < 4) {
            const f32x4 a0 = *(const LAS f32x4*)(L + L_AL + 32 * lg), a1 = *(const LAS f32x4*)(L + L_AL + 32 * lg + 16);
            { f32x2 t = unpk(xp.x) + f32x2{a0[0], a0[1]}; xp.x = cvtpk(t.x, t.y); }
            { f32x2 t = unpk(xp.y) + f32x2{a0[2], a0[3]}; xp.y = cvtpk(t.x, t.y); }
            { f32x2 t = unpk(xp.z) + f32x2{a1[0], a1[1]}; xp.z = cvtpk(t.x, t.y); }
            { f32x2 t = unpk(xp.w) + f32x2{a1[2], a1[3]}; xp.w = cvtpk(t.x, t.y); }
            const v4u h2 = layernorm_pk(layernorm_pk(xp));
            unsigned yv[8], big = 0;
#pragma unroll
            for (int k = 0; k < 4; ++k) {
                yv[2 * k] = *(const LAS unsigned short*)(L + ((h2[k] & 0xFFFFu) << 1));
                yv[2 * k + 1] = *(const LAS unsigned short*)(L + ((h2[k] >> 16) << 1));
                big |= ((h2[k] & 0x7FFF7FFFu) + 0x47004700u) ^ 0x80008000u;
            }
            if (__builtin_expect(__any((big & 0xF800F800u) != 0u), 0)) {
#pragma unroll 1
                for (int k = 0; k < 8; ++k) {
                    unsigned hw = h2[0];
#pragma unroll
                    for (int kk = 1; kk < 4; ++kk) hw = ((k >> 1) == kk) ? h2[kk] : hw;
                    const unsigned u = (k & 1) ? (hw >> 16) : (hw & 0xFFFFu);
                    unsigned long long mk = __ballot(((u & 0x7FFFu) - (unsigned)(TAB_E0 << 7)) >= 2048u);
#pragma unroll 1
                    while (mk) {
                        const int ln = __builtin_ctzll(mk);
                        mk &= mk - 1;
                        const float tv = __uint_as_float((unsigned)__builtin_amdgcn_readlane((int)u, ln) << 16);
                        float a = 0.f, a2 = 0.f;
#pragma unroll 1
                        for (int p = 0; p < 7; ++p) {
                            const LAS unsigned char* q = L + L_MW + 512 * p + 4 * lane;
                            a = mlp_pair(tv, unpk(*(const LAS unsigned*)q), unpk(*(const LAS unsigned*)(q + 3584)), *(const LAS unsigned*)(q + 7168), a);
                            a2 = mlp_pair(tv, unpk(*(const LAS unsigned*)(q + 256)), unpk(*(const LAS unsigned*)(q + 3584 + 256)), *(const LAS unsigned*)(q + 7168 + 256), a2);
                        }
                        const unsigned y = cvtpk(rbf(wave_sum(a + a2)) + b2f, 0.f) & 0xFFFFu;
#pragma unroll
                        for (int kk = 0; kk < 8; ++kk) yv[kk] = (k == kk && lane == ln) ? y : yv[kk];
                    }
                }
            }
#pragma unroll
            for (int k = 0; k < 4; ++k) {
                const f32x2 t = unpk(xp[k]) + f32x2{__uint_as_float(yv[2 * k] << 16), __uint_as_float(yv[2 * k + 1] << 16)};
                xp[k] = cvtpk(t.x, t.y);
            }
            if (layer == 0) {
                stage_q(layernorm_pk(xp), 1);
            } else if (wave == 0) {
                *(v4u*)(out + 8 * lg) = xp;
            }
        }
    }
    asm volatile(".Lpf_mark_a:");
}

extern "C" void kernel_launch(void* const* d_in, const int* in_sizes, int n_in, void* d_out, int out_size, void* d_ws,
                              size_t ws_size, hipStream_t stream) {
    const bf16_t* x = (const bf16_t*)d_in[0];
    const bf16_t* kc = (const bf16_t*)d_in[1];
    const bf16_t* vc = (const bf16_t*)d_in[2];
    const bf16_t* wq = (const bf16_t*)d_in[3];
    const bf16_t* wo = (const bf16_t*)d_in[4];
    const bf16_t* w1 = (const bf16_t*)d_in[6];
    const bf16_t* b1 = (const bf16_t*)d_in[7];
    const bf16_t* w2 = (const bf16_t*)d_in[8];
    const bf16_t* b2 = (const bf16_t*)d_in[9];
    (void)hipFuncSetAttribute((const void*)fused_kernel, hipFuncAttributeMaxDynamicSharedMemorySize, LDS_BYTES);
    fused_kernel<<<NWG, NTHR, LDS_BYTES, stream>>>(x, kc, vc, wq, wo, w1, b1, w2, b2, (bf16_t*)d_out, d_ws);
}
```
